```python
import torch

def _softmax(x, axis=-1):
    return torch.softmax(x, dim=axis)
def _gelu(x):
    return torch.nn.functional.gelu(x, approximate="tanh")
def _silu(x):
    return torch.nn.functional.silu(x)
def _sigmoid(x):
    return torch.sigmoid(x)
def _layernorm(x, axis=-1, eps=1e-5):
    m = x.mean(dim=axis, keepdim=True)
    v = x.var(dim=axis, keepdim=True, unbiased=False)
    return (x - m) / torch.sqrt(v + eps)
def _scatter_add(tgt, idx, src, axis=0):
    return tgt.clone().index_add_(axis, idx, src)
def _segment_sum(src, seg, n):
    out = torch.zeros((n,) + src.shape[1:], dtype=src.dtype)
    return out.index_add_(0, seg, src)
def _pad(x, pw):
    flat = tuple(v for pair in reversed(pw) for v in pair)
    return torch.nn.functional.pad(x, flat)

kernel_name = 'gated_mix__linear_attn_block__dense_activation_squared_relu__prompt'

def setup_inputs():

    _w = (lambda a: (lambda t: t.to(torch.bfloat16) if t.is_floating_point() else t)(torch.as_tensor(a)))
    def _A_setup():
        B, S, D = 1, 2048, 640
        return {
            "x": _w((lambda *s: torch.randn(*s, dtype=torch.bfloat16) * 0.05)(B, S, D)),
            "wqkv": _w((lambda *s: torch.randn(*s, dtype=torch.bfloat16) * 0.8)(D, 3 * D)),
            "wo": _w((lambda *s: torch.randn(*s, dtype=torch.bfloat16) * 0.2)(D, D)),
        }
    _A_in = _A_setup()

    def _B_setup():
        B, S, D, F = 1, 2048, 640, 1536
        return {
            "x": _w((lambda *s: torch.randn(*s, dtype=torch.bfloat16) * 0.05)(B, S, D)),
            "w1": _w((lambda *s: torch.randn(*s, dtype=torch.bfloat16) * 0.1)(D, F)),
            "b1": _w((lambda *s: torch.randn(*s, dtype=torch.bfloat16) * 0.05)(F)),
            "w2": _w((lambda *s: torch.randn(*s, dtype=torch.bfloat16) * 0.05)(F, D)),
            "b2": _w((lambda *s: torch.randn(*s, dtype=torch.bfloat16) * 0.05)(D)),
        }
    _B_in = _B_setup()

    return {
        "A_x": _A_in["x"],
        "A_wqkv": _A_in["wqkv"],
        "A_wo": _A_in["wo"],
        "B_x": _B_in["x"],
        "B_w1": _B_in["w1"],
        "B_b1": _B_in["b1"],
        "B_w2": _B_in["w2"],
        "B_b2": _B_in["b2"],
        "gate_w": _w((lambda *s: torch.randn(*s, dtype=torch.bfloat16) * 0.05)(640) * 16.0),
    }

def reference(A_x, A_wqkv, A_wo, B_x, B_w1, B_b1, B_w2, B_b2, gate_w):
    def _A_ref(x, wqkv, wo):
        B, S, D = x.shape
        H = 32; Dh = D // H
        qkv = (lambda x, *d: x.permute(*d))((x @ wqkv).reshape(B, S, 3, H, Dh), 2, 0, 3, 1, 4)
        q, k, v = qkv[0], qkv[1], qkv[2]
        phi = lambda t: torch.where(t > 0, t + 1, torch.exp(t)) + 1e-6
        pq, pk = phi(q), phi(k)
        kv = torch.einsum("bhsd,bhse->bhde", pk, v)
        z = torch.einsum("bhsd->bhd", pk)
        num = torch.einsum("bhqd,bhde->bhqe", pq, kv)
        den = torch.einsum("bhqd,bhd->bhq", pq, z)[..., None] + 1e-6
        o = num / den
        return (lambda x, *d: x.permute(*d))(o, 0, 2, 1, 3).reshape(B, S, D) @ wo

    def _B_ref(x, w1, b1, w2, b2):
        h = torch.einsum("bsd,df->bsf", x, w1) + b1
        zero = torch.zeros_like(h)
        r = torch.where(h > zero, h, zero)
        h = r * r
        out = torch.einsum("bsf,fd->bsd", h, w2) + b2
        return out

    x = A_x
    g = _sigmoid((x * gate_w).sum(-1, keepdims=True))
    a = _A_ref(x, A_wqkv, A_wo)
    b = _B_ref(x, B_w1, B_b1, B_w2, B_b2)
    return g * a + (1 - g) * b
```

```cpp
#include <hip/hip_runtime.h>
#include <hip/hip_bf16.h>
#include <stdint.h>

typedef __attribute__((ext_vector_type(8))) short bf16x8;
typedef __attribute__((ext_vector_type(4))) short s16x4;
typedef __attribute__((ext_vector_type(4))) float f32x4;
typedef __attribute__((ext_vector_type(2))) float f32x2;
typedef __attribute__((ext_vector_type(2))) __bf16 bf16x2v;
typedef __attribute__((ext_vector_type(4))) unsigned u32x4;
typedef __attribute__((ext_vector_type(2))) unsigned u32x2;
typedef unsigned short u16;
#define LAS __attribute__((address_space(3)))
#define GAS __attribute__((address_space(1)))
#define DI __device__ __forceinline__

constexpr int S_ = 2048, D_ = 640, F_ = 1536;
constexpr int NKS = 68;

constexpr size_t WS_CNT = 0;
constexpr size_t WS_AFRAG = 4096;
constexpr size_t WS_BFRAG = WS_AFRAG + (size_t)128 * NKS * 1024;
constexpr size_t WS_KVP = WS_BFRAG + (size_t)40 * NKS * 1024;
constexpr size_t WS_Z = WS_KVP + (size_t)16 * 32 * 448 * 4;
constexpr size_t WS_G = WS_Z + 8 * 1280;
constexpr size_t WS_END = WS_G + 2048 * 4;

DI unsigned pack2(float a, float b) {
  f32x2 f = {a, b};
  return __builtin_bit_cast(unsigned, __builtin_convertvector(f, bf16x2v));
}
DI float rbf(float a) { return __uint_as_float(pack2(a, a) << 16); }
DI float bflo(unsigned u) { return __uint_as_float(u << 16); }
DI float bfhi(unsigned u) { return __uint_as_float(u & 0xffff0000u); }
DI float phi_f(float acc) {
  float t = rbf(acc);
  float r = t > 0.f ? t + 1.f : __expf(t);
  return rbf(r) + 1e-6f;
}
#define MFMA16(a, b, c) __builtin_amdgcn_mfma_f32_16x16x32_bf16((a), (b), (c), 0, 0, 0)

constexpr int K1_XB = 16384;
constexpr int K1_WROW = 480;
constexpr int K1_WB = 64 * K1_WROW;
constexpr int K1_STAGE = K1_XB + K1_WB;
constexpr int K1_LDS = 3 * K1_STAGE;

DI bf16x8 tr_frag(LAS char* p0, int off) {
  s16x4 lo = __builtin_amdgcn_ds_read_tr16_b64_v4i16((LAS s16x4*)(p0 + off));
  s16x4 hi = __builtin_amdgcn_ds_read_tr16_b64_v4i16((LAS s16x4*)(p0 + off + 16 * K1_WROW));
  return __builtin_shufflevector(lo, hi, 0, 1, 2, 3, 4, 5, 6, 7);
}

template <bool KV>
DI void k1_body(const u16* __restrict__ x, const u16* __restrict__ wmat, int ldw, const u16* __restrict__ b1,
                char* __restrict__ ws, LAS char* smem, int rb, int cb) {
  const int tid = threadIdx.x, lane = tid & 63;
  const int w = __builtin_amdgcn_readfirstlane(tid >> 6);
  const int wr = w >> 1, wc = w & 1;
  const int g = lane >> 4, l15 = lane & 15;

  const unsigned xoff = (unsigned)((rb * 128 + 8 * w + (lane >> 3)) * (D_ * 2) + (((lane & 7) ^ (lane >> 3)) << 4));
  unsigned woff[8];
#pragma unroll
  for (int j = 0; j < 8; ++j) {
    int o = (w + 4 * j) * 1024 + lane * 16;
    if (o >= K1_WB) o = K1_WB - 16;
    int r = o / K1_WROW;
    int c = (o - r * K1_WROW) >> 4;
    if (c > 27) c = 27;
    int k = 32 * (r >> 5) + 8 * ((r >> 2) & 3) + 4 * ((r >> 4) & 1) + (r & 3);
    int col;
    if (KV) {
      int wcc = c >= 14 ? 1 : 0, cc = c - 14 * wcc, hp = 2 * cb + wcc;
      col = cc < 5 ? 640 + 40 * hp + 8 * cc : (cc < 10 ? 1280 + 40 * hp + 8 * (cc - 5) : 32 * hp + 8 * (cc - 10));
    } else {
      if (cb == 8) {
        int wcc = c >= 14 ? 1 : 0, cc = c - 14 * wcc;
        if (cc > 7) cc = 7;
        col = 32 * (16 + 2 * wcc) + 8 * cc;
      } else {
        col = 224 * (cb - 9) + 8 * c;
        if (col > F_ - 8) col = F_ - 8;
      }
    }
    woff[j] = (unsigned)((k * ldw + col) * 2);
  }
  const char* xg = (const char*)x;
  const char* wg = (const char*)wmat;
  const int wstep = 64 * ldw * 2;

  auto stage = [&](int kt, int buf) {
    LAS char* sb = smem + buf * K1_STAGE;
    const char* xs = xg + kt * 128;
    const char* wsrc = wg + (size_t)kt * wstep;
#pragma unroll
    for (int j = 0; j < 4; ++j)
      __builtin_amdgcn_global_load_lds((const GAS void*)(xs + xoff + j * (32 * D_ * 2)), (LAS void*)(sb + (w + 4 * j) * 1024), 16, 0, 0);
#pragma unroll
    for (int j = 0; j < 7; ++j)
      __builtin_amdgcn_global_load_lds((const GAS void*)(wsrc + woff[j]), (LAS void*)(sb + K1_XB + (w + 4 * j) * 1024), 16, 0, 0);
    if (w < 2)
      __builtin_amdgcn_global_load_lds((const GAS void*)(wsrc + woff[7]), (LAS void*)(sb + K1_XB + (w + 28) * 1024), 16, 0, 0);
  };

  f32x4 acc[7][4];
#pragma unroll
  for (int t = 0; t < 7; ++t)
#pragma unroll
    for (int m = 0; m < 4; ++m) acc[t][m] = (f32x4){0.f, 0.f, 0.f, 0.f};

  const int xrd = (wr * 64 + l15) * 128;
  const int xsw = l15 & 7;
  const int wrd = K1_XB + (4 * g + (l15 >> 2)) * K1_WROW + wc * 224 + (l15 & 3) * 8;
  const int wrd4 = K1_XB + (4 * g + (l15 >> 2)) * K1_WROW + wc * 224 + (4 + 5 * (l15 & 3)) * 8;

  auto compute = [&](int buf) {
    LAS char* sb = smem + buf * K1_STAGE;
#pragma unroll
    for (int s = 0; s < 2; ++s) {
      bf16x8 a[4], b[7];
#pragma unroll
      for (int m = 0; m < 4; ++m)
        a[m] = *(LAS bf16x8*)(sb + xrd + m * 2048 + (((4 * s + g) ^ xsw) << 4));
      if (KV) {
        b[0] = tr_frag(sb + wrd, s * 32 * K1_WROW + 0);
        b[1] = tr_frag(sb + wrd, s * 32 * K1_WROW + 40);
        b[2] = tr_frag(sb + wrd, s * 32 * K1_WROW + 80);
        b[3] = tr_frag(sb + wrd, s * 32 * K1_WROW + 120);
        b[4] = tr_frag(sb + wrd4, s * 32 * K1_WROW);
        b[5] = tr_frag(sb + wrd, s * 32 * K1_WROW + 160);
        b[6] = tr_frag(sb + wrd, s * 32 * K1_WROW + 192);
      } else {
#pragma unroll
        for (int t = 0; t < 7; ++t) b[t] = tr_frag(sb + wrd, s * 32 * K1_WROW + 32 * t);
      }
#pragma unroll
      for (int t = 0; t < 7; ++t)
#pragma unroll
        for (int m = 0; m < 4; ++m) {
          if (KV && t < 5) acc[t][m] = MFMA16(a[m], b[t], acc[t][m]);
          else acc[t][m] = MFMA16(b[t], a[m], acc[t][m]);
        }
    }
  };

  stage(0, 0);
  stage(1, 1);
  int buf = 0;
  for (int i = 0; i < 8; ++i) {
    asm volatile("s_waitcnt vmcnt(11)" ::: "memory");
    __builtin_amdgcn_s_barrier();
    asm volatile("" ::: "memory");
    int nb = buf + 2; if (nb >= 3) nb -= 3;
    stage(i + 2, nb);
    compute(buf);
    asm volatile("" ::: "memory");
    buf = buf + 1; if (buf == 3) buf = 0;
  }
  asm volatile("s_waitcnt vmcnt(11)" ::: "memory");
  __builtin_amdgcn_s_barrier();
  asm volatile("" ::: "memory");
  compute(2);
  asm volatile("s_waitcnt vmcnt(0)" ::: "memory");
  __builtin_amdgcn_s_barrier();
  asm volatile("" ::: "memory");
  compute(0);
  asm volatile("" ::: "memory");

  char* afrag = ws + WS_AFRAG;
  const int rt0 = rb * 8 + wr * 4;
  if (KV) {
    const int hp = 2 * cb + wc;
#pragma unroll
    for (int m = 0; m < 4; ++m) {
      u32x4 v;
      v[0] = pack2(phi_f(acc[5][m][0]), phi_f(acc[5][m][1]));
      v[1] = pack2(phi_f(acc[5][m][2]), phi_f(acc[5][m][3]));
      v[2] = pack2(phi_f(acc[6][m][0]), phi_f(acc[6][m][1]));
      v[3] = pack2(phi_f(acc[6][m][2]), phi_f(acc[6][m][3]));
      *(u32x4*)(afrag + ((size_t)(rt0 + m) * NKS + 48 + hp) * 1024 + lane * 16) = v;
    }
    f32x4 R[10];
#pragma unroll
    for (int t = 0; t < 10; ++t) R[t] = (f32x4){0.f, 0.f, 0.f, 0.f};
    const bool iskl = l15 < 8;
    const bf16x8 ones = {0x3f80, 0x3f80, 0x3f80, 0x3f80, 0x3f80, 0x3f80, 0x3f80, 0x3f80};
#pragma unroll
    for (int u = 0; u < 2; ++u) {
      bf16x8 T[5];
#pragma unroll
      for (int t = 0; t < 5; ++t) {
        float e[8];
#pragma unroll
        for (int j = 0; j < 8; ++j) {
          float a = acc[t][2 * u + (j >> 2)][j & 3];
          if (t < 2) e[j] = phi_f(a);
          else if (t < 4) e[j] = a;
          else e[j] = iskl ? phi_f(a) : a;
        }
        u32x4 p = {pack2(e[0], e[1]), pack2(e[2], e[3]), pack2(e[4], e[5]), pack2(e[6], e[7])};
        T[t] = __builtin_bit_cast(bf16x8, p);
      }
      R[0] = MFMA16(T[0], T[2], R[0]);
      R[1] = MFMA16(T[0], T[4], R[1]);
      R[2] = MFMA16(T[4], T[2], R[2]);
      R[3] = MFMA16(T[1], T[3], R[3]);
      R[4] = MFMA16(T[1], T[4], R[4]);
      R[5] = MFMA16(T[4], T[3], R[5]);
      R[6] = MFMA16(T[4], T[4], R[6]);
      R[7] = MFMA16(T[0], ones, R[7]);
      R[8] = MFMA16(T[1], ones, R[8]);
      R[9] = MFMA16(T[4], ones, R[9]);
    }
    LAS char* red = smem + K1_STAGE + wc * 10240;
    if (wr == 1) {
#pragma unroll
      for (int t = 0; t < 10; ++t) *(LAS f32x4*)(red + t * 1024 + lane * 16) = R[t];
    }
    __syncthreads();
    if (wr == 0) {
#pragma unroll
      for (int t = 0; t < 10; ++t) R[t] += *(LAS f32x4*)(red + t * 1024 + lane * 16);
      float* p0 = (float*)(ws + WS_KVP) + ((size_t)rb * 32 + 2 * hp) * 448;
      float* p1 = p0 + 448;
#pragma unroll
      for (int r = 0; r < 4; ++r) {
        const int d = 4 * g + r;
        p0[d * 20 + l15] = R[0][r];
        p1[d * 20 + l15] = R[3][r];
        if (l15 >= 8 && l15 < 12) p0[d * 20 + 8 + l15] = R[1][r];
        if (l15 >= 12) p1[d * 20 + 4 + l15] = R[4][r];
        if (g == 0) p0[(16 + r) * 20 + l15] = R[2][r];
        if (g == 1) p1[(16 + r) * 20 + l15] = R[5][r];
        if (g == 0 && l15 >= 8 && l15 < 12) p0[(16 + r) * 20 + 8 + l15] = R[6][r];
        if (g == 1 && l15 >= 12) p1[(16 + r) * 20 + 4 + l15] = R[6][r];
        if (l15 == 0) { p0[400 + d] = R[7][r]; p1[400 + d] = R[8][r]; }
        if (l15 == 0 && g == 0) p0[416 + r] = R[9][r];
        if (l15 == 0 && g == 1) p1[416 + r] = R[9][r];
      }
    }
  } else {
    if (cb == 8) {
#pragma unroll
      for (int pp = 0; pp < 2; ++pp)
#pragma unroll
        for (int m = 0; m < 4; ++m) {
          u32x4 v;
          v[0] = pack2(phi_f(acc[2 * pp][m][0]), phi_f(acc[2 * pp][m][1]));
          v[1] = pack2(phi_f(acc[2 * pp][m][2]), phi_f(acc[2 * pp][m][3]));
          v[2] = pack2(phi_f(acc[2 * pp + 1][m][0]), phi_f(acc[2 * pp + 1][m][1]));
          v[3] = pack2(phi_f(acc[2 * pp + 1][m][2]), phi_f(acc[2 * pp + 1][m][3]));
          *(u32x4*)(afrag + ((size_t)(rt0 + m) * NKS + 48 + 16 + 2 * wc + pp) * 1024 + lane * 16) = v;
        }
    } else {
      const int T0 = 14 * (cb - 9) + 7 * wc;
#pragma unroll
      for (int t = 0; t < 7; ++t) {
        const int T = T0 + t;
        if (T < 96) {
          u32x2 bb = *(const u32x2*)(b1 + 16 * T + 4 * g);
          const float bv[4] = {bflo(bb[0]), bfhi(bb[0]), bflo(bb[1]), bfhi(bb[1])};
#pragma unroll
          for (int m = 0; m < 4; ++m) {
            float e[4];
#pragma unroll
            for (int r = 0; r < 4; ++r) {
              float hv = rbf(rbf(acc[t][m][r]) + bv[r]);
              hv = hv > 0.f ? hv : 0.f;
              e[r] = hv * hv;
            }
            u32x2 v = {pack2(e[0], e[1]), pack2(e[2], e[3])};
            *(u32x2*)(afrag + ((size_t)(rt0 + m) * NKS + (T >> 1)) * 1024 + lane * 16 + (T & 1) * 8) = v;
          }
        }
      }
    }
    __syncthreads();
  }
}

__global__ void __launch_bounds__(256, 1)
k1_kernel(const u16* __restrict__ x, const u16* __restrict__ wqkv, const u16* __restrict__ w1,
          const u16* __restrict__ b1, const u16* __restrict__ w2, const u16* __restrict__ gate_w,
          char* __restrict__ ws) {
  extern __shared__ __attribute__((aligned(16))) char smem_[];
  LAS char* smem = (LAS char*)smem_;
  const int bid = blockIdx.x, tid = threadIdx.x, lane = tid & 63;
  const int xcd = bid & 7, idx = bid >> 3;
  const int rb = 4 * (xcd >> 1) + (idx >> 3);
  const int cb = 8 * (xcd & 1) + (idx & 7);

  const int ks2 = bid / 5, nq = bid - 5 * ks2;
  u32x4 w2a = {0, 0, 0, 0}, w2b = {0, 0, 0, 0};
  if (bid < 240) {
    const u16* src = w2 + (size_t)(32 * ks2 + (tid >> 4)) * D_ + 128 * nq + 8 * (tid & 15);
    w2a = *(const u32x4*)src;
    w2b = *(const u32x4*)(src + 16 * D_);
  }
  const int grow = rb * 128 + 8 * cb + 2 * (tid >> 6);
  u32x4 gx0 = *(const u32x4*)(x + (size_t)grow * D_ + 8 * lane);
  u32x4 gx1 = *(const u32x4*)(x + (size_t)(grow + 1) * D_ + 8 * lane);
  u32x4 gw0 = *(const u32x4*)(gate_w + 8 * lane);
  u32x4 gx2 = {0, 0, 0, 0}, gw1 = {0, 0, 0, 0};
  if (lane < 32) {
    gx2 = *(const u32x4*)(x + (size_t)(grow + (lane >> 4)) * D_ + 512 + 8 * (lane & 15));
    gw1 = *(const u32x4*)(gate_w + 512 + 8 * (lane & 15));
  }
  if (bid == 255 && tid < 8) *(unsigned*)(ws + WS_CNT + tid * 256) = 0u;

  if (cb < 8) k1_body<true>(x, wqkv, 1920, b1, ws, smem, rb, cb);
  else k1_body<false>(x, cb == 8 ? wqkv : w1, cb == 8 ? 1920 : F_, b1, ws, smem, rb, cb);

  {
    float s0 = 0.f, s1 = 0.f, s2 = 0.f;
#pragma unroll
    for (int j = 0; j < 4; ++j) {
      s0 += rbf(bflo(gx0[j]) * bflo(gw0[j])) + rbf(bfhi(gx0[j]) * bfhi(gw0[j]));
      s1 += rbf(bflo(gx1[j]) * bflo(gw0[j])) + rbf(bfhi(gx1[j]) * bfhi(gw0[j]));
      s2 += rbf(bflo(gx2[j]) * bflo(gw1[j])) + rbf(bfhi(gx2[j]) * bfhi(gw1[j]));
    }
    if (lane < 16) s0 += s2;
    else if (lane < 32) s1 += s2;
#pragma unroll
    for (int o = 32; o >= 1; o >>= 1) {
      s0 += __shfl_xor(s0, o);
      s1 += __shfl_xor(s1, o);
    }
    if (lane < 2) {
      float l = rbf(lane == 0 ? s0 : s1);
      float gv = rbf(1.f / (1.f + __expf(-l)));
      ((float*)(ws + WS_G))[grow + lane] = gv;
    }
  }
  if (bid < 240) {
    LAS char* t = smem + 2 * K1_STAGE;
    constexpr int RS = 288;
    *(LAS u32x4*)(t + (tid >> 4) * RS + (tid & 15) * 16) = w2a;
    *(LAS u32x4*)(t + (16 + (tid >> 4)) * RS + (tid & 15) * 16) = w2b;
    __syncthreads();
    const int w = tid >> 6, g = lane >> 4, l15 = lane & 15;
#pragma unroll
    for (int i = 0; i < 2; ++i) {
      const int ntl = 2 * w + i;
      LAS char* p = t + (4 * g + (l15 >> 2)) * RS + ntl * 32 + (l15 & 3) * 8;
      s16x4 lo = __builtin_amdgcn_ds_read_tr16_b64_v4i16((LAS s16x4*)p);
      s16x4 hi = __builtin_amdgcn_ds_read_tr16_b64_v4i16((LAS s16x4*)(p + 16 * RS));
      bf16x8 v = __builtin_shufflevector(lo, hi, 0, 1, 2, 3, 4, 5, 6, 7);
      *(bf16x8*)(ws + WS_BFRAG + ((size_t)(8 * nq + ntl) * NKS + ks2) * 1024 + lane * 16) = v;
    }
  }
}

constexpr int K2_LDS = 4 * 3 * 2 * 5 * 1024;
constexpr int WIN = 6;

__global__ void __launch_bounds__(256, 1)
k2_kernel(const u16* __restrict__ wo, const u16* __restrict__ b2, char* ws, u16* __restrict__ out) {
  extern __shared__ __attribute__((aligned(16))) char smem_[];
  LAS char* smem = (LAS char*)smem_;
  const int bid = blockIdx.x, tid = threadIdx.x, lane = tid & 63;
  const int w = __builtin_amdgcn_readfirstlane(tid >> 6);
  const int g = lane >> 4, l15 = lane & 15;
  const int xcd = bid & 7, idx = bid >> 3;
  const int rb = 8 * (xcd >> 1) + (idx >> 2);
  const int nu = 4 * (xcd & 1) + (idx & 3);
  const __amdgpu_buffer_rsrc_t wsr = __builtin_amdgcn_make_buffer_rsrc(ws, 0, (int)WS_END, 0x00020000);

  const int ph = bid >> 3, pn = bid & 7;
  {
    LAS float* skv = (LAS float*)smem;
    LAS float* swo = (LAS float*)(smem + 2048);
    if (tid < 105) {
      const float* p = (const float*)(ws + WS_KVP) + (size_t)ph * 448 + 4 * tid;
      f32x4 s = *(const f32x4*)p;
#pragma unroll
      for (int r = 1; r < 16; ++r) s += *(const f32x4*)(p + (size_t)r * 32 * 448);
#pragma unroll
      for (int i = 0; i < 4; ++i) {
        int e = 4 * tid + i;
        float v = rbf(s[i]);
        if (e < 400) { int d = e / 20, ee = e - 20 * d; skv[ee * 20 + d] = v; }
        else skv[e] = v;
      }
    }
    if (tid < 200) {
      int e = tid / 10, c = tid - 10 * e;
      u32x4 v = *(const u32x4*)(wo + (size_t)(20 * ph + e) * D_ + 80 * pn + 8 * c);
#pragma unroll
      for (int j = 0; j < 4; ++j) {
        swo[e * 80 + 8 * c + 2 * j] = bflo(v[j]);
        swo[e * 80 + 8 * c + 2 * j + 1] = bfhi(v[j]);
      }
    }
    __syncthreads();
#pragma unroll
    for (int it = 0; it < 2; ++it) {
      int item = tid + 256 * it;
      if (item < 400) {
        int dq = item / 80, n = item - 80 * dq;
        f32x4 s = {0.f, 0.f, 0.f, 0.f};
#pragma unroll
        for (int e = 0; e < 20; ++e) {
          f32x4 kvv = *(LAS f32x4*)(skv + e * 20 + 4 * dq);
          float wv = swo[e * 80 + n];
          s += kvv * wv;
        }
        int m = 5 * ph + dq;
        int ks = m >> 3, hf = (m >> 2) & 1, gg = m & 3;
        u32x2 v = {pack2(s[0], s[1]), pack2(s[2], s[3])};
        unsigned off = (unsigned)(WS_BFRAG + ((size_t)(5 * pn + (n >> 4)) * NKS + 48 + ks) * 1024 + (16 * gg + (n & 15)) * 16 + hf * 8);
        __builtin_amdgcn_raw_buffer_store_b64(v, wsr, off, 0, 16);
      }
    }
    if (tid < 5) {
      int m = 5 * ph + tid;
      int ks = m >> 3, hf = (m >> 2) & 1, gg = m & 3;
      u32x2 v = {pack2(skv[400 + 4 * tid], skv[401 + 4 * tid]), pack2(skv[402 + 4 * tid], skv[403 + 4 * tid])};
      unsigned off = (unsigned)(WS_Z + pn * 1280 + ((ks * 4 + gg) * 2 + hf) * 8);
      __builtin_amdgcn_raw_buffer_store_b64(v, wsr, off, 0, 16);
    }
    asm volatile("s_waitcnt vmcnt(0)" ::: "memory");
    __syncthreads();
    if (tid == 0)
      __hip_atomic_fetch_add((GAS unsigned*)(ws + WS_CNT + pn * 256), 1u, __ATOMIC_RELAXED, __HIP_MEMORY_SCOPE_AGENT);
  }

  const char* ap = ws + WS_AFRAG + (size_t)(4 * rb) * NKS * 1024 + lane * 16;
  const char* bp = ws + WS_BFRAG + (size_t)(5 * nu) * NKS * 1024 + lane * 16;
  f32x4 accb[4][5], acca[4][5];
#pragma unroll
  for (int m = 0; m < 4; ++m)
#pragma unroll
    for (int c = 0; c < 5; ++c) { accb[m][c] = (f32x4){0.f, 0.f, 0.f, 0.f}; acca[m][c] = (f32x4){0.f, 0.f, 0.f, 0.f}; }
  {
    bf16x8 A[12][4], B[12][5];
    const char* a0 = ap + (size_t)(12 * w) * 1024;
    const char* b0 = bp + (size_t)(12 * w) * 1024;
#pragma unroll
    for (int s = 0; s < WIN; ++s) {
#pragma unroll
      for (int m = 0; m < 4; ++m) A[s][m] = *(const bf16x8*)(a0 + (size_t)m * NKS * 1024 + s * 1024);
#pragma unroll
      for (int c = 0; c < 5; ++c) B[s][c] = *(const bf16x8*)(b0 + (size_t)c * NKS * 1024 + s * 1024);
    }
#pragma unroll
    for (int s = 0; s < 12; ++s) {
      if (s + WIN < 12) {
#pragma unroll
        for (int m = 0; m < 4; ++m) A[s + WIN][m] = *(const bf16x8*)(a0 + (size_t)m * NKS * 1024 + (s + WIN) * 1024);
#pragma unroll
        for (int c = 0; c < 5; ++c) B[s + WIN][c] = *(const bf16x8*)(b0 + (size_t)c * NKS * 1024 + (s + WIN) * 1024);
      }
#pragma unroll
      for (int m = 0; m < 4; ++m)
#pragma unroll
        for (int c = 0; c < 5; ++c) accb[m][c] = MFMA16(B[s][c], A[s][m], accb[m][c]);
    }
  }
  {
    bf16x8 P[5][4];
    const char* a0 = ap + (size_t)(48 + 5 * w) * 1024;
#pragma unroll
    for (int t = 0; t < 5; ++t)
#pragma unroll
      for (int m = 0; m < 4; ++m) P[t][m] = *(const bf16x8*)(a0 + (size_t)m * NKS * 1024 + t * 1024);
    {
      GAS unsigned* cnt = (GAS unsigned*)(ws + WS_CNT + nu * 256);
      unsigned spins = 0;
      while (true) {
        unsigned v = __builtin_amdgcn_readfirstlane(__hip_atomic_load(cnt, __ATOMIC_RELAXED, __HIP_MEMORY_SCOPE_AGENT));
        if (v >= 32u) break;
        if (++spins > (1u << 22)) break;
        __builtin_amdgcn_s_sleep(1);
      }
      asm volatile("" ::: "memory");
    }
    bf16x8 Mf[5][5];
    u32x4 Z[5];
#pragma unroll
    for (int t = 0; t < 5; ++t) {
      Z[t] = __builtin_amdgcn_raw_buffer_load_b128(wsr, (unsigned)(WS_Z + nu * 1280 + ((5 * w + t) * 4 + g) * 16), 0, 16);
#pragma unroll
      for (int c = 0; c < 5; ++c) {
        u32x4 v = __builtin_amdgcn_raw_buffer_load_b128(wsr, (unsigned)(WS_BFRAG + ((size_t)(5 * nu + c) * NKS + 48 + 5 * w + t) * 1024 + lane * 16), 0, 16);
        Mf[t][c] = __builtin_bit_cast(bf16x8, v);
      }
    }
    bf16x8 Zm[3][5];
#pragma unroll
    for (int sg = 0; sg < 3; ++sg) {
      const int hr = ((l15 >> 2) + 4 * (4 * sg + (l15 & 3))) / 5;
#pragma unroll
      for (int t = 0; t < 5; ++t) {
        const bool used = (sg == 0 && t <= 2) || (sg == 1 && t >= 1) || (sg == 2 && t >= 3);
        if (used) {
          const bool k0 = ((8 * t + g) / 5) == hr, k1 = ((8 * t + 4 + g) / 5) == hr;
          u32x4 v = {k0 ? Z[t][0] : 0u, k0 ? Z[t][1] : 0u, k1 ? Z[t][2] : 0u, k1 ? Z[t][3] : 0u};
          Zm[sg][t] = __builtin_bit_cast(bf16x8, v);
        }
      }
    }
#pragma unroll
    for (int m = 0; m < 4; ++m) {
      f32x4 Dn[3];
#pragma unroll
      for (int sg = 0; sg < 3; ++sg) {
        Dn[sg] = (f32x4){0.f, 0.f, 0.f, 0.f};
#pragma unroll
        for (int t = 0; t < 5; ++t) {
          const bool used = (sg == 0 && t <= 2) || (sg == 1 && t >= 1) || (sg == 2 && t >= 3);
          if (used) Dn[sg] = MFMA16(Zm[sg][t], P[t][m], Dn[sg]);
        }
      }
#pragma unroll
      for (int t = 0; t < 5; ++t) {
        u32x4 p = __builtin_bit_cast(u32x4, P[t][m]);
        u32x4 q;
#pragma unroll
        for (int hf = 0; hf < 2; ++hf) {
          const int u = 2 * t + hf;
          float den = rbf(rbf(Dn[u >> 2][u & 3]) + 1e-6f);
          float r = 1.f / den;
          q[2 * hf] = pack2(bflo(p[2 * hf]) * r, bfhi(p[2 * hf]) * r);
          q[2 * hf + 1] = pack2(bflo(p[2 * hf + 1]) * r, bfhi(p[2 * hf + 1]) * r);
        }
        bf16x8 Ap = __builtin_bit_cast(bf16x8, q);
#pragma unroll
        for (int c = 0; c < 5; ++c) acca[m][c] = MFMA16(Mf[t][c], Ap, acca[m][c]);
      }
    }
  }
  __syncthreads();
#pragma unroll
  for (int m = 0; m < 4; ++m) {
    if (m != w) {
      const int src = w < m ? w : w - 1;
      LAS char* p = smem + ((m * 3 + src) * 10) * 1024 + lane * 16;
#pragma unroll
      for (int c = 0; c < 5; ++c) {
        *(LAS f32x4*)(p + c * 1024) = accb[m][c];
        *(LAS f32x4*)(p + (5 + c) * 1024) = acca[m][c];
      }
    }
  }
  __syncthreads();
  {
    f32x4 fb[5], fa[5];
#pragma unroll
    for (int m = 0; m < 4; ++m)
      if (m == w) {
#pragma unroll
        for (int c = 0; c < 5; ++c) { fb[c] = accb[m][c]; fa[c] = acca[m][c]; }
      }
#pragma unroll
    for (int s = 0; s < 3; ++s) {
      LAS char* p = smem + ((w * 3 + s) * 10) * 1024 + lane * 16;
#pragma unroll
      for (int c = 0; c < 5; ++c) {
        fb[c] += *(LAS f32x4*)(p + c * 1024);
        fa[c] += *(LAS f32x4*)(p + (5 + c) * 1024);
      }
    }
    const int row = 64 * rb + 16 * w + l15;
    const float gv = ((const float*)(ws + WS_G))[row];
    const float om = rbf(1.f - gv);
#pragma unroll
    for (int c = 0; c < 5; ++c) {
      const int n = 80 * nu + 16 * c + 4 * g;
      u32x2 bb = *(const u32x2*)(b2 + n);
      const float bv[4] = {bflo(bb[0]), bfhi(bb[0]), bflo(bb[1]), bfhi(bb[1])};
      float e[4];
#pragma unroll
      for (int r = 0; r < 4; ++r) {
        float bfv = rbf(rbf(fb[c][r]) + bv[r]);
        float ga = rbf(gv * rbf(fa[c][r]));
        float gb = rbf(om * bfv);
        e[r] = ga + gb;
      }
      u32x2 v = {pack2(e[0], e[1]), pack2(e[2], e[3])};
      *(u32x2*)(out + (size_t)row * D_ + n) = v;
    }
  }
}

extern "C" void kernel_launch(void* const* d_in, const int* in_sizes, int n_in, void* d_out, int out_size,
                              void* d_ws, size_t ws_size, hipStream_t stream) {
  const u16* x = (const u16*)d_in[0];
  const u16* wqkv = (const u16*)d_in[1];
  const u16* wo = (const u16*)d_in[2];
  const u16* w1 = (const u16*)d_in[4];
  const u16* b1 = (const u16*)d_in[5];
  const u16* w2 = (const u16*)d_in[6];
  const u16* b2 = (const u16*)d_in[7];
  const u16* gw = (const u16*)d_in[8];
  char* ws = (char*)d_ws;
  hipFuncSetAttribute((const void*)k1_kernel, hipFuncAttributeMaxDynamicSharedMemorySize, K1_LDS);
  hipFuncSetAttribute((const void*)k2_kernel, hipFuncAttributeMaxDynamicSharedMemorySize, K2_LDS);
  k1_kernel<<<256, 256, K1_LDS, stream>>>(x, wqkv, w1, b1, w2, gw, ws);
  k2_kernel<<<256, 256, K2_LDS, stream>>>(wo, b2, ws, (u16*)d_out);
}
```

```cpp
#include <hip/hip_runtime.h>
#include <hip/hip_bf16.h>
#include <stdint.h>

typedef __attribute__((ext_vector_type(8))) short bf16x8;
typedef __attribute__((ext_vector_type(4))) short s16x4;
typedef __attribute__((ext_vector_type(4))) float f32x4;
typedef __attribute__((ext_vector_type(2))) float f32x2;
typedef __attribute__((ext_vector_type(2))) __bf16 bf16x2v;
typedef __attribute__((ext_vector_type(4))) unsigned u32x4;
typedef __attribute__((ext_vector_type(2))) unsigned u32x2;
typedef unsigned short u16;
#define LAS __attribute__((address_space(3)))
#define GAS __attribute__((address_space(1)))
#define DI __device__ __forceinline__

constexpr int S_ = 2048, D_ = 640, F_ = 1536;
constexpr int NKS = 68;

constexpr size_t WS_CNT = 0;
constexpr size_t WS_AFRAG = 4096;
constexpr size_t WS_BFRAG = WS_AFRAG + (size_t)128 * NKS * 1024;
constexpr size_t WS_KVP = WS_BFRAG + (size_t)40 * NKS * 1024;
constexpr size_t WS_ZP = WS_KVP + (size_t)16 * 51200;
constexpr size_t WS_G = WS_ZP + 16 * 2560;
constexpr size_t WS_END = WS_G + 2048 * 4;

DI unsigned pack2(float a, float b) {
  f32x2 f = {a, b};
  return __builtin_bit_cast(unsigned, __builtin_convertvector(f, bf16x2v));
}
DI float rbf(float a) { return __uint_as_float(pack2(a, a) << 16); }
DI float bflo(unsigned u) { return __uint_as_float(u << 16); }
DI float bfhi(unsigned u) { return __uint_as_float(u & 0xffff0000u); }
typedef __attribute__((ext_vector_type(2))) short s16x2;
DI unsigned phi2(float a0, float a1) {
  const unsigned p = pack2(a0, a1);
  const float t0 = bflo(p), t1 = bfhi(p);
  const float e0 = __builtin_amdgcn_exp2f(fminf(t0 * 1.44269504f, 0.f));
  const float e1 = __builtin_amdgcn_exp2f(fminf(t1 * 1.44269504f, 0.f));
  return pack2((fmaxf(t0, 0.f) + e0) + 1e-6f, (fmaxf(t1, 0.f) + e1) + 1e-6f);
}
DI unsigned sqrelu2(float a0, float a1) {
  unsigned q = pack2(a0, a1);
  const s16x2 z = {0, 0};
  q = __builtin_bit_cast(unsigned, __builtin_elementwise_max(__builtin_bit_cast(s16x2, q), z));
  const float u0 = bflo(q), u1 = bfhi(q);
  return pack2(u0 * u0, u1 * u1);
}
#define STAMP(kern,i)
#define ITS_DECL
#define ITS(i, k)
#define ITS_DUMP
#define MFMA16(a, b, c) __builtin_amdgcn_mfma_f32_16x16x32_bf16((a), (b), (c), 0, 0, 0)
#define SCHED() __builtin_amdgcn_sched_barrier(0)
#define SGB(mask, n) __builtin_amdgcn_sched_group_barrier(mask, n, 0)
#define INTERLEAVE() do { _Pragma("unroll") for (int q_ = 0; q_ < 18; ++q_) { SGB(0x100, 1); SGB(0x8, 1); } SGB(0x8, 10); } while (0)
#define STMODE 0
#define ST16(p, v) (*(u32x4*)(p) = (v))
#define ST8(p, v) (*(u32x2*)(p) = (v))

constexpr int K1_XB = 16384;
constexpr int K1_WBLK = 1056;
constexpr int K1_WGRP = 8 * K1_WBLK;
constexpr int K1_WB = 32 * K1_WBLK;
constexpr int K1_STAGE = K1_XB + K1_WB;
constexpr int K1_SCR = 3 * K1_STAGE;
constexpr int K1_LDS = K1_SCR + 4 * 3072;

DI void dma16(unsigned lds_off, unsigned voff, const char* gbase) {
  const unsigned lo = __builtin_amdgcn_readfirstlane((unsigned)(uintptr_t)gbase);
  const unsigned hi = __builtin_amdgcn_readfirstlane((unsigned)((uintptr_t)gbase >> 32));
  const unsigned long long gb = ((unsigned long long)hi << 32) | lo;
  const unsigned lo2 = __builtin_amdgcn_readfirstlane(lds_off);
  asm volatile("s_mov_b32 m0, %0\n\ts_nop 3\n\tglobal_load_lds_dwordx4 %1, %2"
               :: "s"(lo2), "v"(voff), "s"(gb) : "memory");
}

DI bf16x8 tr_frag(LAS char* p0, int off) {
  s16x4 lo = __builtin_amdgcn_ds_read_tr16_b64_v4i16((LAS s16x4*)(p0 + off));
  s16x4 hi = __builtin_amdgcn_ds_read_tr16_b64_v4i16((LAS s16x4*)(p0 + off + K1_WGRP));
  return __builtin_shufflevector(lo, hi, 0, 1, 2, 3, 4, 5, 6, 7);
}

template <bool KV>
DI void k1_compute(const u16* __restrict__ b1, char* __restrict__ ws, LAS char* smem, int rb, int cb, int w) {
  const int lane = threadIdx.x & 63;
  const int wr = w >> 1, wc = w & 1;
  const int g = lane >> 4, l15 = lane & 15;

  f32x4 acc[7][4];
#pragma unroll
  for (int t = 0; t < 7; ++t) {
    f32x4 init = {0.f, 0.f, 0.f, 0.f};
    if (!KV) {
      int T = 14 * (cb - 9) + 7 * wc + t;
      T = T < 0 ? 0 : (T > 95 ? 95 : T);
      const u32x2 bb = *(const u32x2*)(b1 + 16 * T + 4 * g);
      if (cb > 8) init = (f32x4){bflo(bb[0]), bfhi(bb[0]), bflo(bb[1]), bfhi(bb[1])};
    }
#pragma unroll
    for (int m = 0; m < 4; ++m) acc[t][m] = init;
  }
  const int xrd = (wr * 64 + l15) * 128;
  const int xsw = l15 & 7;
  const int wrow = K1_XB + (4 * (g & 1) + (l15 >> 2)) * K1_WBLK + (g >> 1) * 512;
  const int ka = 32 * (cb & 1);
  const int wrd = wrow + (KV ? ka + wc * 80 : wc * 224) + (l15 & 3) * 8;
  const int wrdq = wrow + 384 + wc * 64 + (l15 & 3) * 8;
  const int wrd4 = wrow + ka + wc * 80 + 32 + (l15 & 1) * 40 + ((l15 >> 1) & 1) * 192;

  bf16x8 a0[4], b0[7], a1[4], b1f[7];
  auto rd = [&](LAS char* sb, int s, bf16x8 (&a)[4], bf16x8 (&b)[7]) {
#pragma unroll
    for (int m = 0; m < 4; ++m) a[m] = *(LAS bf16x8*)(sb + xrd + m * 2048 + (((4 * s + g) ^ xsw) << 4));
    if (KV) {
      b[0] = tr_frag(sb + wrd, s * 2 * K1_WGRP + 0);
      b[1] = tr_frag(sb + wrd, s * 2 * K1_WGRP + 40);
      b[2] = tr_frag(sb + wrd, s * 2 * K1_WGRP + 192);
      b[3] = tr_frag(sb + wrd, s * 2 * K1_WGRP + 232);
      b[4] = tr_frag(sb + wrd4, s * 2 * K1_WGRP);
      b[5] = tr_frag(sb + wrdq, s * 2 * K1_WGRP);
      b[6] = tr_frag(sb + wrdq, s * 2 * K1_WGRP + 32);
    } else {
#pragma unroll
      for (int t = 0; t < 7; ++t) b[t] = tr_frag(sb + wrd, s * 2 * K1_WGRP + 32 * t);
    }
  };
  auto mm = [&](bf16x8 (&a)[4], bf16x8 (&b)[7]) {
#pragma unroll
    for (int t = 0; t < 7; ++t)
#pragma unroll
      for (int m = 0; m < 4; ++m) {
        if (KV && t < 5) acc[t][m] = MFMA16(a[m], b[t], acc[t][m]);
        else acc[t][m] = MFMA16(b[t], a[m], acc[t][m]);
      }
  };
  __builtin_amdgcn_s_barrier();
  asm volatile("" ::: "memory");
  rd(smem, 0, a0, b0);
  rd(smem, 1, a1, b1f);
  mm(a0, b0);
  int buf = 1;
#pragma unroll 1
  for (int i = 1; i < 10; ++i) {
    asm volatile("s_waitcnt lgkmcnt(0)" ::: "memory");
    __builtin_amdgcn_s_barrier();
    asm volatile("" ::: "memory");
    LAS char* sb = smem + buf * K1_STAGE;
    SCHED();
    rd(sb, 0, a0, b0);
    mm(a1, b1f);
    INTERLEAVE();
    SCHED();
    rd(sb, 1, a1, b1f);
    mm(a0, b0);
    INTERLEAVE();
    SCHED();
    buf = buf + 1; if (buf == 3) buf = 0;
  }
  mm(a1, b1f);
  STAMP(0, 3);

  char* afrag = ws + WS_AFRAG;
  const int rt0 = rb * 8 + wr * 4;
  if (KV) {
    const int hp = 2 * cb + wc;
    f32x4 R[10];
#pragma unroll
    for (int t = 0; t < 10; ++t) R[t] = (f32x4){0.f, 0.f, 0.f, 0.f};
    const bool iskl = l15 < 8;
    const bf16x8 ones = {0x3f80, 0x3f80, 0x3f80, 0x3f80, 0x3f80, 0x3f80, 0x3f80, 0x3f80};
#pragma unroll
    for (int u = 0; u < 2; ++u) {
      bf16x8 T[5];
#pragma unroll
      for (int t = 0; t < 5; ++t) {
        u32x4 p;
#pragma unroll
        for (int j = 0; j < 4; ++j) {
          const float e0 = acc[t][2 * u + (j >> 1)][2 * (j & 1)], e1 = acc[t][2 * u + (j >> 1)][2 * (j & 1) + 1];
          if (t < 2) p[j] = phi2(e0, e1);
          else if (t < 4) p[j] = pack2(e0, e1);
          else p[j] = iskl ? phi2(e0, e1) : pack2(e0, e1);
        }
        T[t] = __builtin_bit_cast(bf16x8, p);
      }
      R[0] = MFMA16(T[0], T[2], R[0]);
      R[1] = MFMA16(T[0], T[4], R[1]);
      R[2] = MFMA16(T[4], T[2], R[2]);
      R[3] = MFMA16(T[1], T[3], R[3]);
      R[4] = MFMA16(T[1], T[4], R[4]);
      R[5] = MFMA16(T[4], T[3], R[5]);
      R[6] = MFMA16(T[4], T[4], R[6]);
      R[7] = MFMA16(T[0], ones, R[7]);
      R[8] = MFMA16(T[1], ones, R[8]);
      R[9] = MFMA16(T[4], ones, R[9]);
    }
    LAS char* red = smem + K1_STAGE + wc * 10240;
    if (wr == 1) {
#pragma unroll
      for (int t = 0; t < 10; ++t) *(LAS f32x4*)(red + t * 1024 + lane * 16) = R[t];
    }
#pragma unroll
    for (int m = 0; m < 4; ++m) {
      u32x4 v;
      v[0] = phi2(acc[5][m][0], acc[5][m][1]);
      v[1] = phi2(acc[5][m][2], acc[5][m][3]);
      v[2] = phi2(acc[6][m][0], acc[6][m][1]);
      v[3] = phi2(acc[6][m][2], acc[6][m][3]);
      ST16(afrag + ((size_t)(rt0 + m) * NKS + 48 + hp) * 1024 + lane * 16, v);
    }
    asm volatile("s_waitcnt lgkmcnt(0)" ::: "memory");
    __builtin_amdgcn_s_barrier();
    asm volatile("" ::: "memory");
    if (wr == 0) {
#pragma unroll
      for (int t = 0; t < 10; ++t) R[t] += *(LAS f32x4*)(red + t * 1024 + lane * 16);
      char* kp = ws + WS_KVP + (size_t)rb * 51200 + (size_t)(10 * hp) * 320;
      *(f32x4*)(kp + g * 320 + l15 * 16) = R[0];
      *(f32x4*)(kp + 1600 + g * 320 + l15 * 16) = R[3];
      if (l15 >= 8) *(f32x4*)(kp + (l15 >= 12 ? 1600 : 0) + g * 320 + (16 + (l15 & 3)) * 16) = l15 >= 12 ? R[4] : R[1];
      if (g < 2) *(f32x4*)(kp + g * 1600 + 4 * 320 + l15 * 16) = g ? R[5] : R[2];
      if (g < 2 && (l15 >> 2) == 2 + g) *(f32x4*)(kp + g * 1600 + 4 * 320 + (16 + (l15 & 3)) * 16) = R[6];
      char* zp = ws + WS_ZP + (size_t)rb * 2560 + (size_t)(10 * hp) * 16;
      if (l15 < 2 || (l15 == 2 && g < 2))
        *(f32x4*)(zp + (l15 == 0 ? g * 16 : (l15 == 1 ? 80 + g * 16 : g * 80 + 64))) = l15 == 0 ? R[7] : (l15 == 1 ? R[8] : R[9]);
    }
  } else {
    if (cb == 8) {
#pragma unroll
      for (int pp = 0; pp < 2; ++pp)
#pragma unroll
        for (int m = 0; m < 4; ++m) {
          u32x4 v;
          v[0] = phi2(acc[2 * pp][m][0], acc[2 * pp][m][1]);
          v[1] = phi2(acc[2 * pp][m][2], acc[2 * pp][m][3]);
          v[2] = phi2(acc[2 * pp + 1][m][0], acc[2 * pp + 1][m][1]);
          v[3] = phi2(acc[2 * pp + 1][m][2], acc[2 * pp + 1][m][3]);
          ST16(afrag + ((size_t)(rt0 + m) * NKS + 48 + 16 + 2 * wc + pp) * 1024 + lane * 16, v);
        }
    } else {
      const int T0 = 14 * (cb - 9) + 7 * wc;
      u32x2 hv[7][4];
#pragma unroll
      for (int t = 0; t < 7; ++t) {
#pragma unroll
        for (int m = 0; m < 4; ++m) {
          hv[t][m][0] = sqrelu2(acc[t][m][0], acc[t][m][1]);
          hv[t][m][1] = sqrelu2(acc[t][m][2], acc[t][m][3]);
        }
      }
      char* base = afrag + ((size_t)rt0 * NKS + (T0 >> 1)) * 1024 + lane * 16;
      if (wc == 0) {
#pragma unroll
        for (int m = 0; m < 4; ++m) {
          char* bm = base + (size_t)m * NKS * 1024;
#pragma unroll
          for (int pp = 0; pp < 3; ++pp) {
            u32x4 v = {hv[2 * pp][m][0], hv[2 * pp][m][1], hv[2 * pp + 1][m][0], hv[2 * pp + 1][m][1]};
            ST16(bm + pp * 1024, v);
          }
          ST8(bm + 3 * 1024, hv[6][m]);
        }
      } else {
        const bool full = T0 + 6 < 96;
#pragma unroll
        for (int m = 0; m < 4; ++m) {
          char* bm = base + (size_t)m * NKS * 1024;
          ST8(bm + 8, hv[0][m]);
#pragma unroll
          for (int pp = 0; pp < 3; ++pp) {
            u32x4 v = {hv[2 * pp + 1][m][0], hv[2 * pp + 1][m][1], hv[2 * pp + 2][m][0], hv[2 * pp + 2][m][1]};
            if (pp < 2 || full) ST16(bm + (pp + 1) * 1024, v);
          }
        }
      }
    }
  }
}

__global__ void __launch_bounds__(512, 2)
k1_kernel(const u16* __restrict__ x, const u16* __restrict__ wqkv, const u16* __restrict__ w1,
          const u16* __restrict__ b1, const u16* __restrict__ w2, const u16* __restrict__ gate_w,
          char* __restrict__ ws) {
  extern __shared__ __attribute__((aligned(16))) char smem_[];
  LAS char* smem = (LAS char*)smem_;
  const int bid = blockIdx.x, tid = threadIdx.x, lane = tid & 63;
  const int w = __builtin_amdgcn_readfirstlane(tid >> 6);
  const int xcd = bid & 7, idx = bid >> 3;
  const int rb = 8 * (xcd >> 2) + (idx >> 2);
  const int cb = 4 * (xcd & 3) + (idx & 3);
  STAMP(0, 0);

  if (w < 4) {
    if (cb < 8) k1_compute<true>(b1, ws, smem, rb, cb, w);
    else k1_compute<false>(b1, ws, smem, rb, cb, w);
    STAMP(0, 4);
    return;
  }
  __builtin_amdgcn_s_setprio(3);
  const int lw = w - 4;
  const unsigned lds0 = (unsigned)(size_t)smem;
  const unsigned xoff = (unsigned)((lane >> 3) * (D_ * 2) + (((lane & 7) ^ (lane >> 3)) << 4));
  const char* xg = (const char*)x + (size_t)(rb * 128 + 8 * lw) * (D_ * 2);
  const int ldw = cb <= 8 ? 1920 : F_;
  const int rl = lane >> 5, c = lane & 31;
  bool wact;
  int col;
  if (cb < 8) {
    const int cs = c - 2 * (cb & 1);
    col = c >= 24 ? 64 * cb + 8 * (c - 24) : (cs < 12 ? 640 + 80 * cb + 8 * cs : 1280 + 80 * cb + 8 * (cs - 12));
    wact = c >= 24 || (cs >= 0 && cs < 10) || (cs >= 12 && cs < 22);
  } else if (cb == 8) {
    const int wcc = c >= 14 ? 1 : 0, cc = c - 14 * wcc;
    col = 512 + 64 * wcc + 8 * cc;
    wact = c < 28 && cc < 8;
  } else {
    col = 224 * (cb - 9) + 8 * c;
    wact = c < 28 && col < F_;
  }
  if (!wact) col = 0;
  const unsigned woff = (unsigned)((rl * 16 * ldw + col) * 2);
  const char* wg = (const char*)(cb <= 8 ? wqkv : w1) + (size_t)lw * ldw * 2;

  auto stage = [&](int kt, int buf) {
    const unsigned sb = lds0 + buf * K1_STAGE;
    const char* xs = xg + kt * 128;
#pragma unroll
    for (int j = 0; j < 4; ++j) dma16(sb + (lw + 4 * j) * 1024, xoff, xs + j * (32 * D_ * 2));
    const char* wsrc = wg + (size_t)kt * 64 * ldw * 2;
    if (wact) {
#pragma unroll
      for (int j = 0; j < 8; ++j) {
        const int kj = 32 * (j >> 2) + 8 * (j & 1) + 4 * ((j >> 1) & 1);
        dma16(sb + K1_XB + (lw + 4 * j) * K1_WBLK, woff, wsrc + (size_t)kj * ldw * 2);
      }
    }
  };
  stage(0, 0);
  stage(1, 1);
  STAMP(0, 1);
  ITS_DECL
  ITS(0, 0);
  asm volatile("s_waitcnt vmcnt(12)" ::: "memory");
  ITS(0, 1);
  __builtin_amdgcn_s_barrier();
  asm volatile("" ::: "memory");
  ITS(0, 2);
  stage(2, 2);
  ITS(0, 3);

  int nb = 0;
#pragma unroll 1
  for (int i = 1; i < 8; ++i) {
    ITS(i, 0);
    asm volatile("s_waitcnt vmcnt(12)" ::: "memory");
    ITS(i, 1);
    __builtin_amdgcn_s_barrier();
    asm volatile("" ::: "memory");
    ITS(i, 2);
    stage(i + 2, nb);
    ITS(i, 3);
    nb = nb + 1; if (nb == 3) nb = 0;
  }
  asm volatile("s_waitcnt vmcnt(12)" ::: "memory");
  __builtin_amdgcn_s_barrier();
  asm volatile("s_waitcnt vmcnt(0)" ::: "memory");
  __builtin_amdgcn_s_barrier();
  asm volatile("" ::: "memory");
  STAMP(0, 2);
  __builtin_amdgcn_s_setprio(0);
  const int u2 = 4 * bid + lw;
  const int ks2 = u2 / 20, np2 = u2 - 20 * ks2;
  u32x4 w2a = {0, 0, 0, 0}, w2b = {0, 0, 0, 0};
  if (u2 < 960) {
    const u16* src = w2 + (size_t)(32 * ks2 + (lane >> 2)) * D_ + 32 * np2 + 8 * (lane & 3);
    w2a = *(const u32x4*)src;
    w2b = *(const u32x4*)(src + 16 * D_);
  }
  const int grow = rb * 128 + 8 * cb + 2 * lw;
  u32x4 gx0 = *(const u32x4*)(x + (size_t)grow * D_ + 8 * lane);
  u32x4 gx1 = *(const u32x4*)(x + (size_t)(grow + 1) * D_ + 8 * lane);
  u32x4 gw0 = *(const u32x4*)(gate_w + 8 * lane);
  u32x4 gx2 = *(const u32x4*)(x + (size_t)(grow + ((lane >> 4) & 1)) * D_ + 512 + 8 * (lane & 15));
  u32x4 gw1 = *(const u32x4*)(gate_w + 512 + 8 * (lane & 15));
  {
    const u32x4 sent = {0xffffffffu, 0xffffffffu, 0xffffffffu, 0xffffffffu};
    if (u2 < 800) {
      const int nt = u2 / 20, ks = u2 - 20 * nt;
      *(u32x4*)(ws + WS_BFRAG + ((size_t)nt * NKS + 48 + ks) * 1024 + lane * 16) = sent;
    }
  }

  {
    float s0 = 0.f, s1 = 0.f, s2 = 0.f;
#pragma unroll
    for (int j = 0; j < 4; ++j) {
      s0 += rbf(bflo(gx0[j]) * bflo(gw0[j])) + rbf(bfhi(gx0[j]) * bfhi(gw0[j]));
      s1 += rbf(bflo(gx1[j]) * bflo(gw0[j])) + rbf(bfhi(gx1[j]) * bfhi(gw0[j]));
      s2 += rbf(bflo(gx2[j]) * bflo(gw1[j])) + rbf(bfhi(gx2[j]) * bfhi(gw1[j]));
    }
    if (lane < 16) s0 += s2;
    else if (lane < 32) s1 += s2;
#pragma unroll
    for (int o = 32; o >= 1; o >>= 1) {
      s0 += __shfl_xor(s0, o);
      s1 += __shfl_xor(s1, o);
    }
    if (lane < 2) {
      float l = rbf(lane == 0 ? s0 : s1);
      float gv = rbf(1.f / (1.f + __expf(-l)));
      ((float*)(ws + WS_G))[grow + lane] = gv;
    }
  }
  if (u2 < 960) {
    LAS char* t = smem + K1_SCR + lw * 3072;
    constexpr int RS = 96;
    *(LAS u32x4*)(t + (lane >> 2) * RS + (lane & 3) * 16) = w2a;
    *(LAS u32x4*)(t + (16 + (lane >> 2)) * RS + (lane & 3) * 16) = w2b;
    asm volatile("s_waitcnt lgkmcnt(0)" ::: "memory");
    const int g = lane >> 4, l15 = lane & 15;
#pragma unroll
    for (int i = 0; i < 2; ++i) {
      LAS char* p = t + (4 * g + (l15 >> 2)) * RS + i * 32 + (l15 & 3) * 8;
      s16x4 lo = __builtin_amdgcn_ds_read_tr16_b64_v4i16((LAS s16x4*)p);
      s16x4 hi = __builtin_amdgcn_ds_read_tr16_b64_v4i16((LAS s16x4*)(p + 16 * RS));
      bf16x8 v = __builtin_shufflevector(lo, hi, 0, 1, 2, 3, 4, 5, 6, 7);
      *(bf16x8*)(ws + WS_BFRAG + ((size_t)(2 * np2 + i) * NKS + ks2) * 1024 + lane * 16) = v;
    }
  }

  ITS_DUMP
  if (cb < 8) __builtin_amdgcn_s_barrier();
  STAMP(0, 4);
}

constexpr int K2_LDS = 8 * 20 * 1024;
#define WINF 3
constexpr int WIN = WINF;
#define MAUX 0

__global__ void __launch_bounds__(512, 2)
k2_kernel(const u16* __restrict__ wo, const u16* __restrict__ b2, char* ws, u16* __restrict__ out) {
  extern __shared__ __attribute__((aligned(16))) char smem_[];
  LAS char* smem = (LAS char*)smem_;
  const int bid = blockIdx.x, tid = threadIdx.x, lane = tid & 63;
  const int w = __builtin_amdgcn_readfirstlane(tid >> 6);
  const int g = lane >> 4, l15 = lane & 15;
  const int xcd = bid & 7, idx = bid >> 3;
  const int rb = 8 * (xcd >> 1) + (idx >> 2);
  const int nu = 4 * (xcd & 1) + (idx & 3);
  const __amdgpu_buffer_rsrc_t wsr = __builtin_amdgcn_make_buffer_rsrc(ws, 0, (int)WS_END, 0x00020000);
  const unsigned lv = lane * 16;
  const unsigned ap = (unsigned)(WS_AFRAG + (size_t)(4 * rb) * NKS * 1024);
#define LDF(soff) __builtin_bit_cast(bf16x8, __builtin_amdgcn_raw_buffer_load_b128(wsr, lv, (soff), 0))
  STAMP(1, 0);
  if (w < 4) {
    f32x4 acc[4][5];
#pragma unroll
    for (int m = 0; m < 4; ++m)
#pragma unroll
      for (int c = 0; c < 5; ++c) acc[m][c] = (f32x4){0.f, 0.f, 0.f, 0.f};
    bf16x8 A[WIN][4], B[WIN][5];
    const unsigned a0 = ap + 12 * w * 1024;
    const unsigned b0 = (unsigned)(WS_BFRAG + (size_t)(5 * nu) * NKS * 1024) + 12 * w * 1024;
#pragma unroll
    for (int s = 0; s < WIN; ++s) {
      const int so = s;
#pragma unroll
      for (int m = 0; m < 4; ++m) A[s][m] = LDF(a0 + (m * NKS + so) * 1024);
#pragma unroll
      for (int c = 0; c < 5; ++c) B[s][c] = LDF(b0 + (c * NKS + so) * 1024);
    }
    SCHED();
#pragma unroll
    for (int s = 0; s < 12; ++s) {
#pragma unroll
      for (int m = 0; m < 4; ++m)
#pragma unroll
        for (int c = 0; c < 5; ++c) acc[m][c] = MFMA16(B[s % WIN][c], A[s % WIN][m], acc[m][c]);
      SCHED();
      if (s + WIN < 12) {
        const int so = s + WIN;
#pragma unroll
        for (int m = 0; m < 4; ++m) A[s % WIN][m] = LDF(a0 + (m * NKS + so) * 1024);
#pragma unroll
        for (int c = 0; c < 5; ++c) B[s % WIN][c] = LDF(b0 + (c * NKS + so) * 1024);
      }
      SCHED();
    }
    STAMP(1, 2);
    LAS char* p = smem + w * 20480 + lane * 16;
#pragma unroll
    for (int m = 0; m < 4; ++m)
#pragma unroll
      for (int c = 0; c < 5; ++c) *(LAS f32x4*)(p + (m * 5 + c) * 1024) = acc[m][c];
  } else {
    __builtin_amdgcn_s_setprio(3);
    const int aw = w - 4;
    const int J = 32 * aw + idx;
    const int ksj = xcd + 8 * (J / 40);
    const bool job = J < 120 && ksj < 20;
    bf16x8 P[5][4];
    const unsigned a0 = ap + (48 + 5 * aw) * 1024;
    f32x4 zq[16];
    const unsigned zo = (unsigned)((40 * aw + (lane < 40 ? lane : 39)) * 16);
#define LOADZP() _Pragma("unroll") for (int r = 0; r < 16; ++r) \
      zq[r] = __builtin_bit_cast(f32x4, __builtin_amdgcn_raw_buffer_load_b128(wsr, zo, (unsigned)(WS_ZP + (size_t)r * 2560), 0))
    if (job) {
      const int ks = ksj;
      const int rem = J % 40, gg = rem / 10, ct = rem - 10 * gg;
      const int hf = lane >> 5, l31 = lane & 31;
      const int m = 8 * ks + 4 * hf + gg, ph = m / 5, dq = m - 5 * ph;
      LAS float* skv = (LAS float*)(smem + w * 20480);
      const unsigned kvo = (unsigned)(m * 320 + (l31 < 20 ? l31 : 19) * 16);
      f32x4 t[16];
#pragma unroll
      for (int r = 0; r < 16; ++r)
        t[r] = __builtin_bit_cast(f32x4, __builtin_amdgcn_raw_buffer_load_b128(wsr, kvo, (unsigned)(WS_KVP + (size_t)r * 51200), 0));
      unsigned wv[20];
#pragma unroll
      for (int e = 0; e < 20; ++e) wv[e] = *(const unsigned*)(wo + (size_t)(20 * ph + e) * D_ + 64 * ct + 2 * l31);
      SCHED();
      LOADZP();
#pragma unroll
      for (int tt = 0; tt < 5; ++tt)
#pragma unroll
        for (int mm = 0; mm < 4; ++mm) P[tt][mm] = LDF(a0 + (mm * NKS + tt) * 1024);
      SCHED();
      const f32x4 s = (((t[0] + t[1]) + (t[2] + t[3])) + ((t[4] + t[5]) + (t[6] + t[7]))) +
                      (((t[8] + t[9]) + (t[10] + t[11])) + ((t[12] + t[13]) + (t[14] + t[15])));
      if (l31 < 20) {
        const unsigned q0 = pack2(s[0], s[1]), q1 = pack2(s[2], s[3]);
        *(LAS f32x4*)(skv + hf * 80 + l31 * 4) = (f32x4){bflo(q0), bfhi(q0), bflo(q1), bfhi(q1)};
      }
      asm volatile("s_waitcnt lgkmcnt(0)" ::: "memory");
      f32x4 o0 = {0.f, 0.f, 0.f, 0.f}, o1 = {0.f, 0.f, 0.f, 0.f};
#pragma unroll
      for (int e = 0; e < 20; ++e) {
        f32x4 kvv = *(LAS f32x4*)(skv + hf * 80 + e * 4);
        o0 += kvv * bflo(wv[e]);
        o1 += kvv * bfhi(wv[e]);
      }
      const auto rx = __builtin_amdgcn_permlane32_swap(pack2(o0[0], o0[1]), pack2(o1[0], o1[1]), false, false);
      const auto ry = __builtin_amdgcn_permlane32_swap(pack2(o0[2], o0[3]), pack2(o1[2], o1[3]), false, false);
      const u32x4 v = {rx[0], ry[0], rx[1], ry[1]};
      const int n = 64 * ct + 2 * l31 + hf;
      __builtin_amdgcn_raw_buffer_store_b128(v, wsr, (unsigned)(WS_BFRAG + ((size_t)(n >> 4) * NKS + 48 + ks) * 1024 + (16 * gg + (n & 15)) * 16), 0, 16);
    } else {
      LOADZP();
#pragma unroll
      for (int tt = 0; tt < 5; ++tt)
#pragma unroll
        for (int mm = 0; mm < 4; ++mm) P[tt][mm] = LDF(a0 + (mm * NKS + tt) * 1024);
    }
    SCHED();
    __builtin_amdgcn_s_setprio(0);
    STAMP(1, 1);
    u32x4 Mf[3][5];
    const unsigned mo = (unsigned)(WS_BFRAG + ((size_t)(5 * nu) * NKS + 48 + 5 * aw) * 1024);
    u32x4 Z[5];
    {
      const f32x4 zs = (((zq[0] + zq[1]) + (zq[2] + zq[3])) + ((zq[4] + zq[5]) + (zq[6] + zq[7]))) +
                       (((zq[8] + zq[9]) + (zq[10] + zq[11])) + ((zq[12] + zq[13]) + (zq[14] + zq[15])));
      LAS char* zl = smem + w * 20480 + 1024;
      if (lane < 40) *(LAS u32x2*)(zl + (((lane >> 3) * 4 + (lane & 3)) * 2 + ((lane >> 2) & 1)) * 8) = (u32x2){pack2(zs[0], zs[1]), pack2(zs[2], zs[3])};
      asm volatile("s_waitcnt lgkmcnt(0)" ::: "memory");
#pragma unroll
      for (int t = 0; t < 5; ++t) Z[t] = *(LAS u32x4*)(zl + (t * 4 + g) * 16);
    }
    STAMP(1, 2);
    {
      f32x4 Dn[4][3];
#pragma unroll
      for (int sg = 0; sg < 3; ++sg) {
        const int hr = ((l15 >> 2) + 4 * (4 * sg + (l15 & 3))) / 5;
#pragma unroll
        for (int m = 0; m < 4; ++m) Dn[m][sg] = (f32x4){0.f, 0.f, 0.f, 0.f};
#pragma unroll
        for (int t = 0; t < 5; ++t) {
          const bool used = (sg == 0 && t <= 2) || (sg == 1 && t >= 1) || (sg == 2 && t >= 3);
          if (used) {
            const bool k0 = ((8 * t + g) / 5) == hr, k1 = ((8 * t + 4 + g) / 5) == hr;
            u32x4 v = {k0 ? Z[t][0] : 0u, k0 ? Z[t][1] : 0u, k1 ? Z[t][2] : 0u, k1 ? Z[t][3] : 0u};
            const bf16x8 zm = __builtin_bit_cast(bf16x8, v);
#pragma unroll
            for (int m = 0; m < 4; ++m) Dn[m][sg] = MFMA16(zm, P[t][m], Dn[m][sg]);
          }
        }
      }
#pragma unroll
      for (int m = 0; m < 4; ++m) {
#pragma unroll
        for (int t = 0; t < 5; ++t) {
          u32x4 p = __builtin_bit_cast(u32x4, P[t][m]);
          u32x4 q;
          const unsigned d1 = pack2(Dn[m][(2 * t) >> 2][(2 * t) & 3], Dn[m][(2 * t + 1) >> 2][(2 * t + 1) & 3]);
          const unsigned d2 = pack2(bflo(d1) + 1e-6f, bfhi(d1) + 1e-6f);
          const float r0 = __builtin_amdgcn_rcpf(bflo(d2)), r1 = __builtin_amdgcn_rcpf(bfhi(d2));
          q[0] = pack2(bflo(p[0]) * r0, bfhi(p[0]) * r0);
          q[1] = pack2(bflo(p[1]) * r0, bfhi(p[1]) * r0);
          q[2] = pack2(bflo(p[2]) * r1, bfhi(p[2]) * r1);
          q[3] = pack2(bflo(p[3]) * r1, bfhi(p[3]) * r1);
          P[t][m] = __builtin_bit_cast(bf16x8, q);
          asm volatile("" : "+v"(P[t][m]));
        }
      }
    }
    SCHED();
    STAMP(1, 6);
    {
      const int pl = lane < 40 ? lane : 39, pt = pl >> 3, pg = (pl >> 1) & 3, pe = pl & 1;
      const unsigned po = mo + ((pe ? 4 : 0) * NKS + pt) * 1024 + (16 * pg + (pe ? 15 : 0)) * 16;
      for (unsigned spins = 0;; ++spins) {
        const unsigned v = __builtin_amdgcn_raw_buffer_load_b32(wsr, po, 0, 16);
        if (__all(v != 0xffffffffu) || spins > (1u << 16)) break;
        __builtin_amdgcn_s_sleep(4);
      }
    }
    STAMP(1, 7);
    __builtin_amdgcn_s_setprio(2);
#pragma unroll
    for (int t = 0; t < 3; ++t)
#pragma unroll
      for (int c = 0; c < 5; ++c) Mf[t][c] = __builtin_amdgcn_raw_buffer_load_b128(wsr, lv, mo + (c * NKS + t) * 1024, 16);
    SCHED();
    f32x4 acc[4][5];
    for (unsigned spins = 0;; ++spins) {
      unsigned mx = 0u;
#pragma unroll
      for (int t = 0; t < 3; ++t)
#pragma unroll
        for (int c = 0; c < 5; ++c) mx = max(max(mx, max(Mf[t][c][0], Mf[t][c][1])), max(Mf[t][c][2], Mf[t][c][3]));
      if (__builtin_expect(__all(mx != 0xffffffffu) || spins > (1u << 16), 1)) break;
      __builtin_amdgcn_s_sleep(8);
#pragma unroll
      for (int t = 0; t < 3; ++t)
#pragma unroll
        for (int c = 0; c < 5; ++c) Mf[t][c] = __builtin_amdgcn_raw_buffer_load_b128(wsr, lv, mo + (c * NKS + t) * 1024, 16);
    }
#pragma unroll
    for (int t = 0; t < 3; ++t) {
#pragma unroll
      for (int m = 0; m < 4; ++m)
#pragma unroll
        for (int c = 0; c < 5; ++c)
          acc[m][c] = MFMA16(__builtin_bit_cast(bf16x8, Mf[t][c]), P[t][m], t == 0 ? ((f32x4){0.f, 0.f, 0.f, 0.f}) : acc[m][c]);
      SCHED();
      if (t < 2) {
#pragma unroll
        for (int c = 0; c < 5; ++c) Mf[t][c] = __builtin_amdgcn_raw_buffer_load_b128(wsr, lv, mo + (c * NKS + t + 3) * 1024, 16);
      }
      SCHED();
    }
    for (unsigned spins = 0;; ++spins) {
      unsigned mx = 0u;
#pragma unroll
      for (int t = 0; t < 2; ++t)
#pragma unroll
        for (int c = 0; c < 5; ++c) mx = max(max(mx, max(Mf[t][c][0], Mf[t][c][1])), max(Mf[t][c][2], Mf[t][c][3]));
      if (__builtin_expect(__all(mx != 0xffffffffu) || spins > (1u << 16), 1)) break;
      __builtin_amdgcn_s_sleep(8);
#pragma unroll
      for (int t = 0; t < 2; ++t)
#pragma unroll
        for (int c = 0; c < 5; ++c) Mf[t][c] = __builtin_amdgcn_raw_buffer_load_b128(wsr, lv, mo + (c * NKS + t + 3) * 1024, 16);
    }
#pragma unroll
    for (int t = 3; t < 5; ++t) {
#pragma unroll
      for (int m = 0; m < 4; ++m)
#pragma unroll
        for (int c = 0; c < 5; ++c) acc[m][c] = MFMA16(__builtin_bit_cast(bf16x8, Mf[t - 3][c]), P[t][m], acc[m][c]);
    }
    STAMP(1, 3);
    LAS char* p = smem + w * 20480 + lane * 16;
#pragma unroll
    for (int m = 0; m < 4; ++m)
#pragma unroll
      for (int c = 0; c < 5; ++c) *(LAS f32x4*)(p + (m * 5 + c) * 1024) = acc[m][c];
  }
  const int fm = w >> 1, c0 = (w & 1) ? 3 : 0, nc = (w & 1) ? 2 : 3;
  const int row = 64 * rb + 16 * fm + l15;
  const float gv = ((const float*)(ws + WS_G))[row];
  u32x2 bb[3];
#pragma unroll
  for (int i = 0; i < 3; ++i) bb[i] = *(const u32x2*)(b2 + 80 * nu + 16 * (c0 + (i < nc ? i : 0)) + 4 * g);
  __syncthreads();
  STAMP(1, 4);
  {
    const float om = rbf(1.f - gv);
#pragma unroll
    for (int i = 0; i < 3; ++i) {
      if (i < nc) {
        const int c = c0 + i;
        LAS char* p = smem + (fm * 5 + c) * 1024 + lane * 16;
        f32x4 fb = (*(LAS f32x4*)(p) + *(LAS f32x4*)(p + 20480)) + (*(LAS f32x4*)(p + 2 * 20480) + *(LAS f32x4*)(p + 3 * 20480));
        f32x4 fa = (*(LAS f32x4*)(p + 4 * 20480) + *(LAS f32x4*)(p + 5 * 20480)) + (*(LAS f32x4*)(p + 6 * 20480) + *(LAS f32x4*)(p + 7 * 20480));
        const int n = 80 * nu + 16 * c + 4 * g;
        const float bv[4] = {bflo(bb[i][0]), bfhi(bb[i][0]), bflo(bb[i][1]), bfhi(bb[i][1])};
        float e[4];
#pragma unroll
        for (int r = 0; r < 4; ++r) {
          float bfv = rbf(rbf(fb[r]) + bv[r]);
          float ga = rbf(gv * rbf(fa[r]));
          float gb = rbf(om * bfv);
          e[r] = ga + gb;
        }
        u32x2 v = {pack2(e[0], e[1]), pack2(e[2], e[3])};
        *(u32x2*)(out + (size_t)row * D_ + n) = v;
      }
    }
  }
  STAMP(1, 5);
}

extern "C" void kernel_launch(void* const* d_in, const int* in_sizes, int n_in, void* d_out, int out_size,
                              void* d_ws, size_t ws_size, hipStream_t stream) {
  const u16* x = (const u16*)d_in[0];
  const u16* wqkv = (const u16*)d_in[1];
  const u16* wo = (const u16*)d_in[2];
  const u16* w1 = (const u16*)d_in[4];
  const u16* b1 = (const u16*)d_in[5];
  const u16* w2 = (const u16*)d_in[6];
  const u16* b2 = (const u16*)d_in[7];
  const u16* gw = (const u16*)d_in[8];
  char* ws = (char*)d_ws;
  (void)hipFuncSetAttribute((const void*)k1_kernel, hipFuncAttributeMaxDynamicSharedMemorySize, K1_LDS);
  (void)hipFuncSetAttribute((const void*)k2_kernel, hipFuncAttributeMaxDynamicSharedMemorySize, K2_LDS);
  k1_kernel<<<256, 512, K1_LDS, stream>>>(x, wqkv, w1, b1, w2, gw, ws);
  k2_kernel<<<256, 512, K2_LDS, stream>>>(wo, b2, ws, (u16*)d_out);
}
```

```cpp
#include <hip/hip_runtime.h>
#include <hip/hip_bf16.h>
#include <stdint.h>

typedef __attribute__((ext_vector_type(8))) short bf16x8;
typedef __attribute__((ext_vector_type(4))) short s16x4;
typedef __attribute__((ext_vector_type(2))) short s16x2;
typedef __attribute__((ext_vector_type(4))) float f32x4;
typedef __attribute__((ext_vector_type(2))) float f32x2;
typedef __attribute__((ext_vector_type(2))) __bf16 bf16x2v;
typedef __attribute__((ext_vector_type(4))) unsigned u32x4;
typedef __attribute__((ext_vector_type(2))) unsigned u32x2;
typedef unsigned short u16;
typedef unsigned long long u64;
#define LAS __attribute__((address_space(3)))
#define GAS __attribute__((address_space(1)))
#define DI __device__ __forceinline__

constexpr int S_ = 2048, D_ = 640, F_ = 1536;
constexpr int NKS = 68;

constexpr unsigned WS_EPOCH = 0;
constexpr unsigned WS_F1 = 1024;
constexpr unsigned WS_FG = 3072 + 8192;
constexpr unsigned WS_F0 = 4096;
constexpr unsigned WS_AFRAG = 16384;
constexpr unsigned WS_MFRAG = WS_AFRAG + 128 * NKS * 1024;
constexpr unsigned WS_KVP = WS_MFRAG + 40 * 20 * 1024;
constexpr unsigned WS_ZP = WS_KVP + 16 * 51200;
constexpr unsigned WS_G = WS_ZP + 16 * 2560;
constexpr unsigned WS_END = WS_G + 2048 * 4;

DI unsigned pack2(float a, float b) {
  f32x2 f = {a, b};
  return __builtin_bit_cast(unsigned, __builtin_convertvector(f, bf16x2v));
}
DI float rbf(float a) { return __uint_as_float(pack2(a, a) << 16); }
DI float bflo(unsigned u) { return __uint_as_float(u << 16); }
DI float bfhi(unsigned u) { return __uint_as_float(u & 0xffff0000u); }
DI unsigned phi2(float a0, float a1) {
  const unsigned p = pack2(a0, a1);
  const float t0 = bflo(p), t1 = bfhi(p);
  const float e0 = __builtin_amdgcn_exp2f(fminf(t0 * 1.44269504f, 0.f));
  const float e1 = __builtin_amdgcn_exp2f(fminf(t1 * 1.44269504f, 0.f));
  return pack2((fmaxf(t0, 0.f) + e0) + 1e-6f, (fmaxf(t1, 0.f) + e1) + 1e-6f);
}
DI f32x4 sel4(bool c, f32x4 a, f32x4 b) {
  f32x4 r;
#pragma unroll
  for (int i = 0; i < 4; ++i) { float x = a[i], y = b[i]; asm volatile("" : "+v"(x), "+v"(y)); r[i] = c ? x : y; }
  return r;
}
DI unsigned sqrelu2(float a0, float a1) {
  unsigned q = pack2(a0, a1);
  const s16x2 z = {0, 0};
  q = __builtin_bit_cast(unsigned, __builtin_elementwise_max(__builtin_bit_cast(s16x2, q), z));
  const float u0 = bflo(q), u1 = bfhi(q);
  return pack2(u0 * u0, u1 * u1);
}
#define STAMP(kern,i)
#define MFMA16(a, b, c) __builtin_amdgcn_mfma_f32_16x16x32_bf16((a), (b), (c), 0, 0, 0)
#define SCHED() __builtin_amdgcn_sched_barrier(0)
#define SGB(mask, n) __builtin_amdgcn_sched_group_barrier(mask, n, 0)
#define INTERLEAVE() do { _Pragma("unroll") for (int q_ = 0; q_ < 18; ++q_) { SGB(0x100, 1); SGB(0x8, 1); } SGB(0x8, 10); } while (0)
#define BAR() do { asm volatile("s_waitcnt lgkmcnt(0)" ::: "memory"); __builtin_amdgcn_s_barrier(); asm volatile("" ::: "memory"); } while (0)
#define DRAIN() asm volatile("s_waitcnt vmcnt(0)" ::: "memory")
#define ST16(off, v) __builtin_amdgcn_raw_buffer_store_b128(__builtin_bit_cast(u32x4, v), wsr, (unsigned)(off), 0, 16)
#define ST8(off, v) __builtin_amdgcn_raw_buffer_store_b64((v), wsr, (unsigned)(off), 0, 16)
#define LD16(voff, soff) __builtin_amdgcn_raw_buffer_load_b128(wsr, (unsigned)(voff), (unsigned)(soff), 16)
constexpr unsigned SPIN_CAP = 1u << 18;
#define POLL_SLEEP 8

constexpr int K1_XB = 16384;
constexpr int K1_WBLK = 1056;
constexpr int K1_WGRP = 8 * K1_WBLK;
constexpr int K1_WB = 32 * K1_WBLK;
constexpr int K1_STAGE = K1_XB + K1_WB;
constexpr int RING_A = K1_STAGE;
constexpr int KVRED = 132096;
constexpr int ASCR = 152576;
constexpr int CTRL = 162816;
constexpr int LDS_BYTES = 163840;

DI void dma16(unsigned lds_off, unsigned voff, const char* gbase) {
  const unsigned lo = __builtin_amdgcn_readfirstlane((unsigned)(uintptr_t)gbase);
  const unsigned hi = __builtin_amdgcn_readfirstlane((unsigned)((uintptr_t)gbase >> 32));
  const u64 gb = ((u64)hi << 32) | lo;
  const unsigned lo2 = __builtin_amdgcn_readfirstlane(lds_off);
  asm volatile("s_mov_b32 m0, %0\n\ts_nop 3\n\tglobal_load_lds_dwordx4 %1, %2"
               :: "s"(lo2), "v"(voff), "s"(gb) : "memory");
}

DI bf16x8 tr_frag(LAS char* p0, int off, int step) {
  s16x4 lo = __builtin_amdgcn_ds_read_tr16_b64_v4i16((LAS s16x4*)(p0 + off));
  s16x4 hi = __builtin_amdgcn_ds_read_tr16_b64_v4i16((LAS s16x4*)(p0 + off + step));
  return __builtin_shufflevector(lo, hi, 0, 1, 2, 3, 4, 5, 6, 7);
}

DI void wait_flags(char* ws, unsigned off, bool care, u64 tag) {
  GAS u64* p = (GAS u64*)(ws + off);
  for (unsigned spins = 0;; ++spins) {
    const u64 v = __hip_atomic_load(p, __ATOMIC_RELAXED, __HIP_MEMORY_SCOPE_AGENT);
    if (__all(!care || v == tag) || spins > SPIN_CAP) break;
    __builtin_amdgcn_s_sleep(POLL_SLEEP);
  }
  __builtin_amdgcn_fence(__ATOMIC_ACQUIRE, "wavefront");
  asm volatile("" ::: "memory");
}

DI void lds_signal(LAS char* smem, int word, int lane) {
  asm volatile("s_waitcnt lgkmcnt(0)" ::: "memory");
  if (lane == 0) __hip_atomic_fetch_add((LAS unsigned*)(smem + CTRL) + word, 1u, __ATOMIC_RELAXED, __HIP_MEMORY_SCOPE_WORKGROUP);
}
DI void lds_wait(LAS char* smem, int word, unsigned n) {
  while (__builtin_amdgcn_readfirstlane(__hip_atomic_load((LAS unsigned*)(smem + CTRL) + word, __ATOMIC_RELAXED, __HIP_MEMORY_SCOPE_WORKGROUP)) < n)
    __builtin_amdgcn_s_sleep(1);
  asm volatile("" ::: "memory");
}
DI void arrive_publish(LAS char* smem, int word, char* ws, unsigned flag_off, u64 tag, unsigned n, int lane) {
  if (lane == 0) {
    const unsigned old = __hip_atomic_fetch_add((LAS unsigned*)(smem + CTRL) + word, 1u, __ATOMIC_RELAXED, __HIP_MEMORY_SCOPE_WORKGROUP);
    if (old == n - 1) {
      __hip_atomic_store((GAS u64*)(ws + flag_off), tag, __ATOMIC_RELAXED, __HIP_MEMORY_SCOPE_AGENT);
    }
  }
}

template <bool KV>
DI void p1_compute(const u16* __restrict__ b1, char* ws, __amdgpu_buffer_rsrc_t wsr, LAS char* smem, int rb, int cb, int w, u64 tag, u64 (&f0)[4]) {
  const int lane = threadIdx.x & 63;
  const int wr = w >> 1, wc = w & 1;
  const int g = lane >> 4, l15 = lane & 15;

  u32x2 bb[7];
  if (!KV) {
#pragma unroll
    for (int t = 0; t < 7; ++t) {
      int T = 14 * (cb - 9) + 7 * wc + t;
      T = T < 0 ? 0 : (T > 95 ? 95 : T);
      bb[t] = *(const u32x2*)(b1 + 16 * T + 4 * g);
    }
  }
  const int xrd = (wr * 64 + l15) * 128;
  const int xsw = l15 & 7;
  const int wrow = K1_XB + (4 * (g & 1) + (l15 >> 2)) * K1_WBLK + (g >> 1) * 512;
  const int ka = 32 * (cb & 1);
  const int wrd = wrow + (KV ? ka + wc * 80 : wc * 224) + (l15 & 3) * 8;
  const int wrdq = wrow + 384 + wc * 64 + (l15 & 3) * 8;
  const int wrd4 = wrow + ka + wc * 80 + 32 + (l15 & 1) * 40 + ((l15 >> 1) & 1) * 192;

  f32x4 acc[7][4];
  bf16x8 a0[4], b0[7], a1[4], b1f[7];
  auto rd = [&](LAS char* sb, int s, bf16x8 (&a)[4], bf16x8 (&b)[7]) {
#pragma unroll
    for (int m = 0; m < 4; ++m) a[m] = *(LAS bf16x8*)(sb + xrd + m * 2048 + (((4 * s + g) ^ xsw) << 4));
    if (KV) {
      b[0] = tr_frag(sb + wrd, s * 2 * K1_WGRP + 0, K1_WGRP);
      b[1] = tr_frag(sb + wrd, s * 2 * K1_WGRP + 40, K1_WGRP);
      b[2] = tr_frag(sb + wrd, s * 2 * K1_WGRP + 192, K1_WGRP);
      b[3] = tr_frag(sb + wrd, s * 2 * K1_WGRP + 232, K1_WGRP);
      b[4] = tr_frag(sb + wrd4, s * 2 * K1_WGRP, K1_WGRP);
      b[5] = tr_frag(sb + wrdq, s * 2 * K1_WGRP, K1_WGRP);
      b[6] = tr_frag(sb + wrdq, s * 2 * K1_WGRP + 32, K1_WGRP);
    } else {
#pragma unroll
      for (int t = 0; t < 7; ++t) b[t] = tr_frag(sb + wrd, s * 2 * K1_WGRP + 32 * t, K1_WGRP);
    }
  };
  auto mm = [&](bf16x8 (&a)[4], bf16x8 (&b)[7]) {
#pragma unroll
    for (int t = 0; t < 7; ++t)
#pragma unroll
      for (int m = 0; m < 4; ++m) {
        if (KV && t < 5) acc[t][m] = MFMA16(a[m], b[t], acc[t][m]);
        else acc[t][m] = MFMA16(b[t], a[m], acc[t][m]);
      }
  };
  BAR();
  rd(smem, 0, a0, b0);
  rd(smem, 1, a1, b1f);
  DRAIN();
  arrive_publish(smem, 5, ws, WS_F0 + blockIdx.x * 8, tag, 4, lane);
#pragma unroll
  for (int t = 0; t < 7; ++t) {
    f32x4 init = {0.f, 0.f, 0.f, 0.f};
    if (!KV && cb > 8) init = (f32x4){bflo(bb[t][0]), bfhi(bb[t][0]), bflo(bb[t][1]), bfhi(bb[t][1])};
#pragma unroll
    for (int m = 0; m < 4; ++m) acc[t][m] = init;
  }
  mm(a0, b0);
  int buf = 1;
#pragma unroll 1
  for (int i = 1; i < 10; ++i) {
    BAR();
    LAS char* sb = smem + buf * K1_STAGE;
    SCHED();
    rd(sb, 0, a0, b0);
    mm(a1, b1f);
    INTERLEAVE();
    SCHED();
    rd(sb, 1, a1, b1f);
    mm(a0, b0);
    INTERLEAVE();
    SCHED();
    buf = buf + 1; if (buf == 3) buf = 0;
  }
  lds_signal(smem, 2, lane);
#pragma unroll
  for (int i = 0; i < 4; ++i) f0[i] = __hip_atomic_load((GAS u64*)(ws + WS_F0) + 64 * i + lane, __ATOMIC_RELAXED, __HIP_MEMORY_SCOPE_AGENT);
  mm(a1, b1f);
  STAMP(0, 3);

  const int rt0 = rb * 8 + wr * 4;
  if (KV) {
    const int hp = 2 * cb + wc;
    f32x4 R[10];
#pragma unroll
    for (int t = 0; t < 10; ++t) R[t] = (f32x4){0.f, 0.f, 0.f, 0.f};
    const bool iskl = l15 < 8;
    const bf16x8 ones = {0x3f80, 0x3f80, 0x3f80, 0x3f80, 0x3f80, 0x3f80, 0x3f80, 0x3f80};
#pragma unroll
    for (int u = 0; u < 2; ++u) {
      bf16x8 T[5];
#pragma unroll
      for (int t = 0; t < 5; ++t) {
        u32x4 p;
#pragma unroll
        for (int j = 0; j < 4; ++j) {
          const float e0 = acc[t][2 * u + (j >> 1)][2 * (j & 1)], e1 = acc[t][2 * u + (j >> 1)][2 * (j & 1) + 1];
          if (t < 2) p[j] = phi2(e0, e1);
          else if (t < 4) p[j] = pack2(e0, e1);
          else p[j] = iskl ? phi2(e0, e1) : pack2(e0, e1);
        }
        T[t] = __builtin_bit_cast(bf16x8, p);
      }
      R[0] = MFMA16(T[0], T[2], R[0]);
      R[1] = MFMA16(T[0], T[4], R[1]);
      R[2] = MFMA16(T[4], T[2], R[2]);
      R[3] = MFMA16(T[1], T[3], R[3]);
      R[4] = MFMA16(T[1], T[4], R[4]);
      R[5] = MFMA16(T[4], T[3], R[5]);
      R[6] = MFMA16(T[4], T[4], R[6]);
      R[7] = MFMA16(T[0], ones, R[7]);
      R[8] = MFMA16(T[1], ones, R[8]);
      R[9] = MFMA16(T[4], ones, R[9]);
    }
    LAS char* red = smem + KVRED + wc * 10240;
    if (wr == 1) {
#pragma unroll
      for (int t = 0; t < 10; ++t) *(LAS f32x4*)(red + t * 1024 + lane * 16) = R[t];
      lds_signal(smem, 3 + wc, lane);
    }
#pragma unroll
    for (int m = 0; m < 4; ++m) {
      u32x4 v;
      v[0] = phi2(acc[5][m][0], acc[5][m][1]);
      v[1] = phi2(acc[5][m][2], acc[5][m][3]);
      v[2] = phi2(acc[6][m][0], acc[6][m][1]);
      v[3] = phi2(acc[6][m][2], acc[6][m][3]);
      ST16(WS_AFRAG + ((rt0 + m) * NKS + 48 + hp) * 1024 + lane * 16, v);
    }
    if (wr == 0) {
      lds_wait(smem, 3 + wc, 1);
#pragma unroll
      for (int t = 0; t < 10; ++t) R[t] += *(LAS f32x4*)(red + t * 1024 + lane * 16);
      const unsigned kp = WS_KVP + rb * 51200 + (10 * hp) * 320;
      ST16(kp + g * 320 + l15 * 16, R[0]);
      ST16(kp + 1600 + g * 320 + l15 * 16, R[3]);
      if (l15 >= 8) ST16(kp + (l15 >= 12 ? 1600 : 0) + g * 320 + (16 + (l15 & 3)) * 16, sel4(l15 >= 12, R[4], R[1]));
      if (g < 2) ST16(kp + g * 1600 + 4 * 320 + l15 * 16, sel4(g != 0, R[5], R[2]));
      if (g < 2 && (l15 >> 2) == 2 + g) ST16(kp + g * 1600 + 4 * 320 + (16 + (l15 & 3)) * 16, R[6]);
      const unsigned zp = WS_ZP + rb * 2560 + (10 * hp) * 16;
      if (l15 < 2 || (l15 == 2 && g < 2))
        ST16(zp + (l15 == 0 ? g * 16 : (l15 == 1 ? 80 + g * 16 : g * 80 + 64)), sel4(l15 == 0, R[7], sel4(l15 == 1, R[8], R[9])));
    }
  } else {
    if (cb == 8) {
#pragma unroll
      for (int pp = 0; pp < 2; ++pp)
#pragma unroll
        for (int m = 0; m < 4; ++m) {
          u32x4 v;
          v[0] = phi2(acc[2 * pp][m][0], acc[2 * pp][m][1]);
          v[1] = phi2(acc[2 * pp][m][2], acc[2 * pp][m][3]);
          v[2] = phi2(acc[2 * pp + 1][m][0], acc[2 * pp + 1][m][1]);
          v[3] = phi2(acc[2 * pp + 1][m][2], acc[2 * pp + 1][m][3]);
          ST16(WS_AFRAG + ((rt0 + m) * NKS + 48 + 16 + 2 * wc + pp) * 1024 + lane * 16, v);
        }
    } else {
      const int T0 = 14 * (cb - 9) + 7 * wc;
      const unsigned base = WS_AFRAG + (rt0 * NKS + (T0 >> 1)) * 1024 + lane * 16;
      const bool full = T0 + 6 < 96;
#pragma unroll
      for (int m = 0; m < 4; ++m) {
        u32x2 hv[7];
#pragma unroll
        for (int t = 0; t < 7; ++t) {
          hv[t][0] = sqrelu2(acc[t][m][0], acc[t][m][1]);
          hv[t][1] = sqrelu2(acc[t][m][2], acc[t][m][3]);
        }
        const unsigned bm = base + m * NKS * 1024;
        if (wc == 0) {
#pragma unroll
          for (int pp = 0; pp < 3; ++pp) {
            u32x4 v = {hv[2 * pp][0], hv[2 * pp][1], hv[2 * pp + 1][0], hv[2 * pp + 1][1]};
            ST16(bm + pp * 1024, v);
          }
          ST8(bm + 3 * 1024, hv[6]);
        } else {
          ST8(bm + 8, hv[0]);
#pragma unroll
          for (int pp = 0; pp < 3; ++pp) {
            u32x4 v = {hv[2 * pp + 1][0], hv[2 * pp + 1][1], hv[2 * pp + 2][0], hv[2 * pp + 2][1]};
            if (pp < 2 || full) ST16(bm + (pp + 1) * 1024, v);
          }
        }
      }
    }
  }
}

#define WINF 4
constexpr int WIN = WINF;
static_assert(WIN <= 6, "ring fills must be older than the h loads of the same step");
constexpr int fops(int it) { return (it + 6 < 12 ? 5 : 0) + (it + WIN < 12 ? 4 : 0); }
constexpr int nwait(int s) {
  int n = 0;
  if (s < WIN) { n += 4 * (WIN - 1 - s); for (int it = 0; it < s; ++it) n += fops(it); }
  else { for (int it = s - WIN + 1; it < s; ++it) n += fops(it); }
  return n;
}
template <int N> DI void waitv() { asm volatile("s_waitcnt vmcnt(%0)" :: "n"(N) : "memory"); }

__global__ void __launch_bounds__(512, 2)
fused_kernel(const u16* __restrict__ x, const u16* __restrict__ wqkv, const u16* __restrict__ wo,
             const u16* __restrict__ w1, const u16* __restrict__ b1, const u16* __restrict__ w2,
             const u16* __restrict__ b2, const u16* __restrict__ gate_w, char* ws, u16* __restrict__ out) {
  extern __shared__ __attribute__((aligned(16))) char smem_[];
  LAS char* smem = (LAS char*)smem_;
  const int bid = blockIdx.x, tid = threadIdx.x, lane = tid & 63;
  const int w = __builtin_amdgcn_readfirstlane(tid >> 6);
  const int g = lane >> 4, l15 = lane & 15;
  const int xcd = bid & 7, idx = bid >> 3;
  const int rb = 8 * (xcd >> 2) + (idx >> 2);
  const int cb = 4 * (xcd & 3) + (idx & 3);
  const int rb2 = 4 * xcd + (idx >> 3);
  const int nu = idx & 7;
  const __amdgpu_buffer_rsrc_t wsr = __builtin_amdgcn_make_buffer_rsrc(ws, 0, (int)WS_END, 0x00020000);
  const unsigned lv = lane * 16;
  const unsigned ap = WS_AFRAG + (4 * rb2) * NKS * 1024;
  const unsigned lds0 = (unsigned)(size_t)smem;
  STAMP(0, 0);
  f32x4 acc[4][5];

  if (w < 4) {
    if (tid < 8) ((LAS unsigned*)(smem + CTRL))[tid] = 0u;
    const u64 tag = __hip_atomic_load((GAS u64*)(ws + WS_EPOCH), __ATOMIC_RELAXED, __HIP_MEMORY_SCOPE_AGENT) + 1ull;
    const int aw = w;
    const int J = 32 * aw + idx;
    const int ks = xcd + 8 * (J / 40);
    const bool job = J < 120 && ks < 20;
    const int gg = (J % 40) / 10, ct = J % 10;
    const int hf = lane >> 5, l31 = lane & 31;
    const unsigned mst = WS_MFRAG + (((64 * ct + 2 * l31 + hf) >> 4) * 20 + ks) * 1024 + (16 * gg + ((2 * l31 + hf) & 15)) * 16;
    {
      const u32x4 sent = {0xffffffffu, 0xffffffffu, 0xffffffffu, 0xffffffffu};
      if (job) ST16(mst, sent);
      if (cb < 8 && w < 2) {
        const unsigned kp = WS_KVP + rb * 51200 + (10 * (2 * cb + w)) * 320;
#pragma unroll
        for (int i = 0; i < 3; ++i) ST16(kp + i * 1024 + lv, sent);
        if (lane < 8) ST16(kp + 3072 + lv, sent);
        if (lane < 10) ST16(WS_ZP + rb * 2560 + (10 * (2 * cb + w)) * 16 + lv, sent);
      }
    }
    u64 f0[4];
    if (cb < 8) p1_compute<true>(b1, ws, wsr, smem, rb, cb, w, tag, f0);
    else p1_compute<false>(b1, ws, wsr, smem, rb, cb, w, tag, f0);
    STAMP(0, 4);
    const int m = 8 * ks + 4 * hf + gg, ph = m / 5;
    unsigned wv[20];
    if (job) {
#pragma unroll
      for (int e = 0; e < 20; ++e) wv[e] = *(const unsigned*)(wo + (size_t)(20 * ph + e) * D_ + 64 * ct + 2 * l31);
    }
    DRAIN();
    arrive_publish(smem, 0, ws, WS_F1 + (rb * 16 + cb) * 8, tag, 4, lane);
    STAMP(0, 5);
    if (!__all(f0[0] == tag && f0[1] == tag && f0[2] == tag && f0[3] == tag)) {
#pragma unroll
      for (int i = 0; i < 4; ++i) wait_flags(ws, WS_F0 + (64 * i + lane) * 8, true, tag);
    }
    asm volatile("" ::: "memory");
    if (job) {
      LAS float* skv = (LAS float*)(smem + ASCR + w * 2560);
      for (unsigned spins = 0;; ++spins) {
        const unsigned v = __builtin_amdgcn_raw_buffer_load_b32(wsr, WS_KVP + (l31 & 15) * 51200 + m * 320, 0, 16);
        if (__all(v != 0xffffffffu) || spins > SPIN_CAP) break;
        __builtin_amdgcn_s_sleep(POLL_SLEEP);
      }
      const unsigned kvo = (unsigned)(m * 320 + (l31 < 20 ? l31 : 19) * 16);
      u32x4 t[16];
      for (unsigned spins = 0;; ++spins) {
#pragma unroll
        for (int r = 0; r < 16; ++r) t[r] = LD16(kvo, WS_KVP + r * 51200);
        unsigned mx = 0u;
#pragma unroll
        for (int r = 0; r < 16; ++r) mx = max(max(mx, max(t[r][0], t[r][1])), max(t[r][2], t[r][3]));
        if (__builtin_expect(__all(mx != 0xffffffffu) || spins > SPIN_CAP, 1)) break;
        __builtin_amdgcn_s_sleep(POLL_SLEEP);
      }
#define F4(x) __builtin_bit_cast(f32x4, x)
      const f32x4 s = (((F4(t[0]) + F4(t[1])) + (F4(t[2]) + F4(t[3]))) + ((F4(t[4]) + F4(t[5])) + (F4(t[6]) + F4(t[7])))) +
                      (((F4(t[8]) + F4(t[9])) + (F4(t[10]) + F4(t[11]))) + ((F4(t[12]) + F4(t[13])) + (F4(t[14]) + F4(t[15]))));
      if (l31 < 20) {
        const unsigned q0 = pack2(s[0], s[1]), q1 = pack2(s[2], s[3]);
        *(LAS f32x4*)(skv + hf * 80 + l31 * 4) = (f32x4){bflo(q0), bfhi(q0), bflo(q1), bfhi(q1)};
      }
      asm volatile("s_waitcnt lgkmcnt(0)" ::: "memory");
      f32x4 o0 = {0.f, 0.f, 0.f, 0.f}, o1 = {0.f, 0.f, 0.f, 0.f};
#pragma unroll
      for (int e = 0; e < 20; ++e) {
        f32x4 kvv = *(LAS f32x4*)(skv + hf * 80 + e * 4);
        o0 += kvv * bflo(wv[e]);
        o1 += kvv * bfhi(wv[e]);
      }
      const auto rx = __builtin_amdgcn_permlane32_swap(pack2(o0[0], o0[1]), pack2(o1[0], o1[1]), false, false);
      const auto ry = __builtin_amdgcn_permlane32_swap(pack2(o0[2], o0[3]), pack2(o1[2], o1[3]), false, false);
      const u32x4 v = {rx[0], ry[0], rx[1], ry[1]};
      ST16(mst, v);
    }
    STAMP(0, 6);
    wait_flags(ws, WS_F1 + ((rb2 >> 1) * 16 + (lane < 9 ? lane : 0)) * 8, true, tag);
    bf16x8 P[5][4];
    u32x4 Z[5];
    {
      u32x4 zq[16];
      const unsigned zo = (unsigned)((40 * aw + (lane < 40 ? lane : 39)) * 16);
#pragma unroll
      for (int r = 0; r < 16; ++r) zq[r] = LD16(zo, WS_ZP + r * 2560);
      const unsigned a0 = ap + (48 + 5 * aw) * 1024;
#pragma unroll
      for (int tt = 0; tt < 5; ++tt)
#pragma unroll
        for (int mm = 0; mm < 4; ++mm) P[tt][mm] = __builtin_bit_cast(bf16x8, LD16(lv, a0 + (mm * NKS + tt) * 1024));
      SCHED();
      for (unsigned spins = 0;; ++spins) {
        unsigned mx = 0u;
#pragma unroll
        for (int r = 0; r < 16; ++r) mx = max(max(mx, max(zq[r][0], zq[r][1])), max(zq[r][2], zq[r][3]));
        if (__builtin_expect(__all(mx != 0xffffffffu) || spins > SPIN_CAP, 1)) break;
        __builtin_amdgcn_s_sleep(POLL_SLEEP);
#pragma unroll
        for (int r = 0; r < 16; ++r) zq[r] = LD16(zo, WS_ZP + r * 2560);
      }
      const f32x4 zs = (((F4(zq[0]) + F4(zq[1])) + (F4(zq[2]) + F4(zq[3]))) + ((F4(zq[4]) + F4(zq[5])) + (F4(zq[6]) + F4(zq[7])))) +
                       (((F4(zq[8]) + F4(zq[9])) + (F4(zq[10]) + F4(zq[11]))) + ((F4(zq[12]) + F4(zq[13])) + (F4(zq[14]) + F4(zq[15]))));
      LAS char* zl = smem + ASCR + w * 2560 + 1024;
      if (lane < 40) *(LAS u32x2*)(zl + (((lane >> 3) * 4 + (lane & 3)) * 2 + ((lane >> 2) & 1)) * 8) = (u32x2){pack2(zs[0], zs[1]), pack2(zs[2], zs[3])};
      asm volatile("s_waitcnt lgkmcnt(0)" ::: "memory");
#pragma unroll
      for (int t = 0; t < 5; ++t) Z[t] = *(LAS u32x4*)(zl + (t * 4 + g) * 16);
    }
    {
      f32x4 Dn[4][3];
#pragma unroll
      for (int sg = 0; sg < 3; ++sg) {
        const int hr = ((l15 >> 2) + 4 * (4 * sg + (l15 & 3))) / 5;
#pragma unroll
        for (int m = 0; m < 4; ++m) Dn[m][sg] = (f32x4){0.f, 0.f, 0.f, 0.f};
#pragma unroll
        for (int t = 0; t < 5; ++t) {
          const bool used = (sg == 0 && t <= 2) || (sg == 1 && t >= 1) || (sg == 2 && t >= 3);
          if (used) {
            const bool k0 = ((8 * t + g) / 5) == hr, k1 = ((8 * t + 4 + g) / 5) == hr;
            u32x4 v = {k0 ? Z[t][0] : 0u, k0 ? Z[t][1] : 0u, k1 ? Z[t][2] : 0u, k1 ? Z[t][3] : 0u};
            const bf16x8 zm = __builtin_bit_cast(bf16x8, v);
#pragma unroll
            for (int m = 0; m < 4; ++m) Dn[m][sg] = MFMA16(zm, P[t][m], Dn[m][sg]);
          }
        }
      }
#pragma unroll
      for (int m = 0; m < 4; ++m) {
#pragma unroll
        for (int t = 0; t < 5; ++t) {
          u32x4 p = __builtin_bit_cast(u32x4, P[t][m]);
          u32x4 q;
          const unsigned d1 = pack2(Dn[m][(2 * t) >> 2][(2 * t) & 3], Dn[m][(2 * t + 1) >> 2][(2 * t + 1) & 3]);
          const unsigned d2 = pack2(bflo(d1) + 1e-6f, bfhi(d1) + 1e-6f);
          const float r0 = __builtin_amdgcn_rcpf(bflo(d2)), r1 = __builtin_amdgcn_rcpf(bfhi(d2));
          q[0] = pack2(bflo(p[0]) * r0, bfhi(p[0]) * r0);
          q[1] = pack2(bflo(p[1]) * r0, bfhi(p[1]) * r0);
          q[2] = pack2(bflo(p[2]) * r1, bfhi(p[2]) * r1);
          q[3] = pack2(bflo(p[3]) * r1, bfhi(p[3]) * r1);
          P[t][m] = __builtin_bit_cast(bf16x8, q);
          asm volatile("" : "+v"(P[t][m]));
        }
      }
    }
    SCHED();
    STAMP(0, 7);
    {
      const unsigned mo = WS_MFRAG + ((5 * nu) * 20 + 5 * aw) * 1024;
      {
        const int pl = lane < 40 ? lane : 39, pt = pl >> 3, pg = (pl >> 1) & 3, pe = pl & 1;
        const unsigned po = mo + ((pe ? 4 : 0) * 20 + pt) * 1024 + (16 * pg + (pe ? 15 : 0)) * 16;
        for (unsigned spins = 0;; ++spins) {
          const unsigned v = __builtin_amdgcn_raw_buffer_load_b32(wsr, po, 0, 16);
          if (__all(v != 0xffffffffu) || spins > SPIN_CAP) break;
          __builtin_amdgcn_s_sleep(POLL_SLEEP);
        }
      }
      STAMP(1, 1);
      u32x4 Mf[3][5];
      for (unsigned spins = 0;; ++spins) {
#pragma unroll
        for (int t = 0; t < 3; ++t)
#pragma unroll
          for (int c = 0; c < 5; ++c) Mf[t][c] = LD16(lv, mo + (c * 20 + t) * 1024);
        unsigned mx = 0u;
#pragma unroll
        for (int t = 0; t < 3; ++t)
#pragma unroll
          for (int c = 0; c < 5; ++c) mx = max(max(mx, max(Mf[t][c][0], Mf[t][c][1])), max(Mf[t][c][2], Mf[t][c][3]));
        if (__builtin_expect(__all(mx != 0xffffffffu) || spins > SPIN_CAP, 1)) break;
        __builtin_amdgcn_s_sleep(POLL_SLEEP);
      }
#pragma unroll
      for (int t = 0; t < 3; ++t) {
#pragma unroll
        for (int mm = 0; mm < 4; ++mm)
#pragma unroll
          for (int c = 0; c < 5; ++c)
            acc[mm][c] = MFMA16(__builtin_bit_cast(bf16x8, Mf[t][c]), P[t][mm], t == 0 ? ((f32x4){0.f, 0.f, 0.f, 0.f}) : acc[mm][c]);
        SCHED();
        if (t < 2) {
#pragma unroll
          for (int c = 0; c < 5; ++c) Mf[t][c] = LD16(lv, mo + (c * 20 + t + 3) * 1024);
        }
        SCHED();
      }
      for (unsigned spins = 0;; ++spins) {
        unsigned mx = 0u;
#pragma unroll
        for (int t = 0; t < 2; ++t)
#pragma unroll
          for (int c = 0; c < 5; ++c) mx = max(max(mx, max(Mf[t][c][0], Mf[t][c][1])), max(Mf[t][c][2], Mf[t][c][3]));
        if (__builtin_expect(__all(mx != 0xffffffffu) || spins > SPIN_CAP, 1)) break;
        __builtin_amdgcn_s_sleep(POLL_SLEEP);
#pragma unroll
        for (int t = 0; t < 2; ++t)
#pragma unroll
          for (int c = 0; c < 5; ++c) Mf[t][c] = LD16(lv, mo + (c * 20 + t + 3) * 1024);
      }
#pragma unroll
      for (int t = 3; t < 5; ++t)
#pragma unroll
        for (int mm = 0; mm < 4; ++mm)
#pragma unroll
          for (int c = 0; c < 5; ++c) acc[mm][c] = MFMA16(__builtin_bit_cast(bf16x8, Mf[t - 3][c]), P[t][mm], acc[mm][c]);
    }
    STAMP(1, 2);
  } else {
    __builtin_amdgcn_s_setprio(3);
    const int lw = w - 4;
    {
      const unsigned xoff = (unsigned)((lane >> 3) * (D_ * 2) + (((lane & 7) ^ (lane >> 3)) << 4));
      const char* xg = (const char*)x + (size_t)(rb * 128 + 8 * lw) * (D_ * 2);
      const int ldw = cb <= 8 ? 1920 : F_;
      const int rl = lane >> 5, c = lane & 31;
      bool wact;
      int col;
      if (cb < 8) {
        const int cs = c - 2 * (cb & 1);
        col = c >= 24 ? 64 * cb + 8 * (c - 24) : (cs < 12 ? 640 + 80 * cb + 8 * cs : 1280 + 80 * cb + 8 * (cs - 12));
        wact = c >= 24 || (cs >= 0 && cs < 10) || (cs >= 12 && cs < 22);
      } else if (cb == 8) {
        const int wcc = c >= 14 ? 1 : 0, cc = c - 14 * wcc;
        col = 512 + 64 * wcc + 8 * cc;
        wact = c < 28 && cc < 8;
      } else {
        col = 224 * (cb - 9) + 8 * c;
        wact = c < 28 && col < F_;
      }
      if (!wact) col = 0;
      const unsigned woff = (unsigned)((rl * 16 * ldw + col) * 2);
      const char* wg = (const char*)(cb <= 8 ? wqkv : w1) + (size_t)lw * ldw * 2;
      auto stage = [&](int kt, int buf) {
        const unsigned sb = lds0 + buf * K1_STAGE;
        const char* xs = xg + kt * 128;
#pragma unroll
        for (int j = 0; j < 4; ++j) dma16(sb + (lw + 4 * j) * 1024, xoff, xs + j * (32 * D_ * 2));
        const char* wsrc = wg + (size_t)kt * 64 * ldw * 2;
        if (wact) {
#pragma unroll
          for (int j = 0; j < 8; ++j) {
            const int kj = 32 * (j >> 2) + 8 * (j & 1) + 4 * ((j >> 1) & 1);
            dma16(sb + K1_XB + (lw + 4 * j) * K1_WBLK, woff, wsrc + (size_t)kj * ldw * 2);
          }
        }
      };
      stage(0, 0);
      stage(1, 1);
      STAMP(0, 1);
      int nb = 2;
#pragma unroll 1
      for (int i = 0; i < 8; ++i) {
        asm volatile("s_waitcnt vmcnt(12)" ::: "memory");
        __builtin_amdgcn_s_barrier();
        asm volatile("" ::: "memory");
        stage(i + 2, nb);
        nb = nb + 1; if (nb == 3) nb = 0;
      }
      asm volatile("s_waitcnt vmcnt(12)" ::: "memory");
      __builtin_amdgcn_s_barrier();
      asm volatile("" ::: "memory");
    }
    const int grow = rb * 128 + 8 * cb + 2 * lw;
    const u32x4 gx0 = *(const u32x4*)(x + (size_t)grow * D_ + 8 * lane);
    const u32x4 gx1 = *(const u32x4*)(x + (size_t)(grow + 1) * D_ + 8 * lane);
    const u32x4 gw0 = *(const u32x4*)(gate_w + 8 * lane);
    const u32x4 gx2 = *(const u32x4*)(x + (size_t)(grow + ((lane >> 4) & 1)) * D_ + 512 + 8 * (lane & 15));
    const u32x4 gw1 = *(const u32x4*)(gate_w + 512 + 8 * (lane & 15));
    const u64 tag = __hip_atomic_load((GAS u64*)(ws + WS_EPOCH), __ATOMIC_RELAXED, __HIP_MEMORY_SCOPE_AGENT) + 1ull;
    SCHED();
    asm volatile("s_waitcnt vmcnt(0)" ::: "memory");
    __builtin_amdgcn_s_barrier();
    asm volatile("" ::: "memory");
    STAMP(0, 2);
    SCHED();
    unsigned w2o[5];
#pragma unroll
    for (int i = 0; i < 5; ++i) {
      const int q = 64 * i + lane, r = q / 10, c = q - 10 * r;
      w2o[i] = (unsigned)(r * (D_ * 2) + c * 16);
    }
    const char* w2g = (const char*)w2 + (size_t)(12 * lw) * 32 * (D_ * 2) + nu * 160;
    auto slot_off = [&](int sl) { return sl < 4 ? RING_A + (4 * lw + sl) * 5120 : (2 * lw + sl - 4) * 5120; };
    auto fill = [&](int s) {
      const unsigned d = lds0 + slot_off(s % 6);
#pragma unroll
      for (int i = 0; i < 5; ++i) dma16(d + i * 1024, w2o[i], w2g + (size_t)s * 32 * (D_ * 2));
    };
#pragma unroll
    for (int s = 0; s < 4; ++s) fill(s);
    lds_wait(smem, 2, 4);
    fill(4);
    fill(5);
    STAMP(0, 3);
    __builtin_amdgcn_s_setprio(0);
    SCHED();
    {
      float s0 = 0.f, s1 = 0.f, s2 = 0.f;
#pragma unroll
      for (int j = 0; j < 4; ++j) {
        s0 += rbf(bflo(gx0[j]) * bflo(gw0[j])) + rbf(bfhi(gx0[j]) * bfhi(gw0[j]));
        s1 += rbf(bflo(gx1[j]) * bflo(gw0[j])) + rbf(bfhi(gx1[j]) * bfhi(gw0[j]));
        s2 += rbf(bflo(gx2[j]) * bflo(gw1[j])) + rbf(bfhi(gx2[j]) * bfhi(gw1[j]));
      }
      if (lane < 16) s0 += s2;
      else if (lane < 32) s1 += s2;
#pragma unroll
      for (int o = 32; o >= 1; o >>= 1) {
        s0 += __shfl_xor(s0, o);
        s1 += __shfl_xor(s1, o);
      }
      if (lane < 2) {
        float l = rbf(lane == 0 ? s0 : s1);
        float gv = rbf(1.f / (1.f + __expf(-l)));
        __builtin_amdgcn_raw_buffer_store_b32(__float_as_uint(gv), wsr, WS_G + (grow + lane) * 4, 0, 16);
      }
    }
    DRAIN();
    arrive_publish(smem, 1, ws, WS_FG + (rb * 16 + cb) * 8, tag, 4, lane);
    STAMP(0, 4);
    wait_flags(ws, WS_F1 + ((rb2 >> 1) * 16 + 9 + (lane < 7 ? lane : 0)) * 8, true, tag);
    STAMP(0, 5);
    u32x4 A[WIN][4];
    const unsigned a0 = ap + 12 * lw * 1024;
#define LDH(dst, soff) asm volatile("buffer_load_dwordx4 %0, %1, %2, %3 offen sc1" : "=v"(dst) : "v"(lv), "s"(wsr), "s"((unsigned)(soff)) : "memory")
#pragma unroll
    for (int s = 0; s < WIN; ++s)
#pragma unroll
      for (int m = 0; m < 4; ++m) LDH(A[s][m], a0 + (m * NKS + s) * 1024);
    const int brd = (4 * g + (l15 >> 2)) * 160 + (l15 & 3) * 8;
#define FSTEP(s) do { \
      waitv<nwait(s)>(); \
      asm volatile("" : "+v"(A[(s) % WIN][0]), "+v"(A[(s) % WIN][1]), "+v"(A[(s) % WIN][2]), "+v"(A[(s) % WIN][3])); \
      LAS char* sl = smem + slot_off((s) % 6) + brd; \
      bf16x8 B[5]; \
      _Pragma("unroll") for (int c = 0; c < 5; ++c) B[c] = tr_frag(sl, 32 * c, 16 * 160); \
      _Pragma("unroll") for (int c = 0; c < 5; ++c) \
        _Pragma("unroll") for (int m = 0; m < 4; ++m) \
          acc[m][c] = MFMA16(B[c], __builtin_bit_cast(bf16x8, A[(s) % WIN][m]), (s) == 0 ? ((f32x4){0.f, 0.f, 0.f, 0.f}) : acc[m][c]); \
      SCHED(); \
      if ((s) + 6 < 12) { asm volatile("s_waitcnt lgkmcnt(0)" ::: "memory"); fill((s) + 6); } \
      if ((s) + WIN < 12) { _Pragma("unroll") for (int m = 0; m < 4; ++m) LDH(A[(s) % WIN][m], a0 + (m * NKS + (s) + WIN) * 1024); } \
      SCHED(); \
    } while (0)
    FSTEP(0); FSTEP(1); FSTEP(2); FSTEP(3); FSTEP(4); FSTEP(5);
    FSTEP(6); FSTEP(7); FSTEP(8); FSTEP(9); FSTEP(10); FSTEP(11);
    STAMP(0, 6);
    wait_flags(ws, WS_FG + ((rb2 >> 1) * 16 + 8 * (rb2 & 1) + (lane & 7)) * 8, true, tag);
    if (bid == 0 && lw == 0) {
#pragma unroll
      for (int i = 0; i < 4; ++i) {
        wait_flags(ws, WS_F1 + (64 * i + lane) * 8, true, tag);
        wait_flags(ws, WS_FG + (64 * i + lane) * 8, true, tag);
      }
      if (lane == 0) __hip_atomic_store((GAS u64*)(ws + WS_EPOCH), tag, __ATOMIC_RELAXED, __HIP_MEMORY_SCOPE_AGENT);
    }
  }
  BAR();
  {
    LAS char* p = smem + w * 20480 + lane * 16;
#pragma unroll
    for (int m = 0; m < 4; ++m)
#pragma unroll
      for (int c = 0; c < 5; ++c) *(LAS f32x4*)(p + (m * 5 + c) * 1024) = acc[m][c];
  }
  const int fm = w >> 1, c0 = (w & 1) ? 3 : 0, nc = (w & 1) ? 2 : 3;
  const int row = 64 * rb2 + 16 * fm + l15;
  const float gv = __uint_as_float(__builtin_amdgcn_raw_buffer_load_b32(wsr, WS_G + row * 4, 0, 16));
  u32x2 bb[3];
#pragma unroll
  for (int i = 0; i < 3; ++i) bb[i] = *(const u32x2*)(b2 + 80 * nu + 16 * (c0 + (i < nc ? i : 0)) + 4 * g);
  BAR();
  STAMP(1, 4);
  {
    const float om = rbf(1.f - gv);
#pragma unroll
    for (int i = 0; i < 3; ++i) {
      if (i < nc) {
        const int c = c0 + i;
        LAS char* p = smem + (fm * 5 + c) * 1024 + lane * 16;
        f32x4 fa = (*(LAS f32x4*)(p) + *(LAS f32x4*)(p + 20480)) + (*(LAS f32x4*)(p + 2 * 20480) + *(LAS f32x4*)(p + 3 * 20480));
        f32x4 fb = (*(LAS f32x4*)(p + 4 * 20480) + *(LAS f32x4*)(p + 5 * 20480)) + (*(LAS f32x4*)(p + 6 * 20480) + *(LAS f32x4*)(p + 7 * 20480));
        const int n = 80 * nu + 16 * c + 4 * g;
        const float bv[4] = {bflo(bb[i][0]), bfhi(bb[i][0]), bflo(bb[i][1]), bfhi(bb[i][1])};
        float e[4];
#pragma unroll
        for (int r = 0; r < 4; ++r) {
          float bfv = rbf(rbf(fb[r]) + bv[r]);
          float ga = rbf(gv * rbf(fa[r]));
          float gb = rbf(om * bfv);
          e[r] = ga + gb;
        }
        u32x2 v = {pack2(e[0], e[1]), pack2(e[2], e[3])};
        *(u32x2*)(out + (size_t)row * D_ + n) = v;
      }
    }
  }
  STAMP(1, 5);
}

extern "C" void kernel_launch(void* const* d_in, const int* in_sizes, int n_in, void* d_out, int out_size,
                              void* d_ws, size_t ws_size, hipStream_t stream) {
  const u16* x = (const u16*)d_in[0];
  const u16* wqkv = (const u16*)d_in[1];
  const u16* wo = (const u16*)d_in[2];
  const u16* w1 = (const u16*)d_in[4];
  const u16* b1 = (const u16*)d_in[5];
  const u16* w2 = (const u16*)d_in[6];
  const u16* b2 = (const u16*)d_in[7];
  const u16* gw = (const u16*)d_in[8];
  (void)hipFuncSetAttribute((const void*)fused_kernel, hipFuncAttributeMaxDynamicSharedMemorySize, LDS_BYTES);
  fused_kernel<<<256, 512, LDS_BYTES, stream>>>(x, wqkv, wo, w1, b1, w2, b2, gw, (char*)d_ws, (u16*)d_out);
}
```

```cpp
#include <hip/hip_runtime.h>
#include <hip/hip_bf16.h>
#include <stdint.h>

typedef __attribute__((ext_vector_type(8))) short bf16x8;
typedef __attribute__((ext_vector_type(4))) short s16x4;
typedef __attribute__((ext_vector_type(4))) float f32x4;
typedef __attribute__((ext_vector_type(2))) float f32x2;
typedef __attribute__((ext_vector_type(2))) __bf16 bf16x2v;
typedef __attribute__((ext_vector_type(4))) unsigned u32x4;
typedef __attribute__((ext_vector_type(2))) unsigned u32x2;
typedef unsigned short u16;
#define LAS __attribute__((address_space(3)))
#define GAS __attribute__((address_space(1)))
#define DI __device__ __forceinline__

constexpr int S_ = 2048, D_ = 640, F_ = 1536;
constexpr int NKS = 68;

constexpr size_t WS_CNT = 0;
constexpr size_t WS_AFRAG = 4096;
constexpr size_t WS_BFRAG = WS_AFRAG + (size_t)128 * NKS * 1024;
constexpr size_t WS_KVP = WS_BFRAG + (size_t)40 * NKS * 1024;
constexpr size_t WS_ZP = WS_KVP + (size_t)16 * 51200;
constexpr size_t WS_G = WS_ZP + 16 * 2560;
constexpr size_t WS_END = WS_G + 2048 * 4;

DI unsigned pack2(float a, float b) {
  f32x2 f = {a, b};
  return __builtin_bit_cast(unsigned, __builtin_convertvector(f, bf16x2v));
}
DI float rbf(float a) { return __uint_as_float(pack2(a, a) << 16); }
DI float bflo(unsigned u) { return __uint_as_float(u << 16); }
DI float bfhi(unsigned u) { return __uint_as_float(u & 0xffff0000u); }
typedef __attribute__((ext_vector_type(2))) short s16x2;
DI unsigned phi2(float a0, float a1) {
  const unsigned p = pack2(a0, a1);
  const float t0 = bflo(p), t1 = bfhi(p);
  const float e0 = __builtin_amdgcn_exp2f(fminf(t0 * 1.44269504f, 0.f));
  const float e1 = __builtin_amdgcn_exp2f(fminf(t1 * 1.44269504f, 0.f));
  return pack2((fmaxf(t0, 0.f) + e0) + 1e-6f, (fmaxf(t1, 0.f) + e1) + 1e-6f);
}
DI unsigned sqrelu2(float a0, float a1) {
  unsigned q = pack2(a0, a1);
  const s16x2 z = {0, 0};
  q = __builtin_bit_cast(unsigned, __builtin_elementwise_max(__builtin_bit_cast(s16x2, q), z));
  const float u0 = bflo(q), u1 = bfhi(q);
  return pack2(u0 * u0, u1 * u1);
}
#define STAMP(kern,i)
#define ITS_DECL
#define ITS(i, k)
#define ITS_DUMP
#define MFMA16(a, b, c) __builtin_amdgcn_mfma_f32_16x16x32_bf16((a), (b), (c), 0, 0, 0)
#define SCHED() __builtin_amdgcn_sched_barrier(0)
#define SGB(mask, n) __builtin_amdgcn_sched_group_barrier(mask, n, 0)
#define INTERLEAVE() do { _Pragma("unroll") for (int q_ = 0; q_ < 18; ++q_) { SGB(0x100, 1); SGB(0x8, 1); } SGB(0x8, 10); } while (0)
#define STMODE 0
#define ST16(p, v) (*(u32x4*)(p) = (v))
#define ST8(p, v) (*(u32x2*)(p) = (v))

constexpr int K1_XB = 16384;
constexpr int K1_WBLK = 1056;
constexpr int K1_WGRP = 8 * K1_WBLK;
constexpr int K1_WB = 32 * K1_WBLK;
constexpr int K1_STAGE = K1_XB + K1_WB;
constexpr int K1_SCR = 3 * K1_STAGE;
constexpr int K1_LDS = K1_SCR + 4 * 3072;

DI void dma16(unsigned lds_off, unsigned voff, const char* gbase) {
  const unsigned lo = __builtin_amdgcn_readfirstlane((unsigned)(uintptr_t)gbase);
  const unsigned hi = __builtin_amdgcn_readfirstlane((unsigned)((uintptr_t)gbase >> 32));
  const unsigned long long gb = ((unsigned long long)hi << 32) | lo;
  const unsigned lo2 = __builtin_amdgcn_readfirstlane(lds_off);
  asm volatile("s_mov_b32 m0, %0\n\ts_nop 3\n\tglobal_load_lds_dwordx4 %1, %2"
               :: "s"(lo2), "v"(voff), "s"(gb) : "memory");
}

DI bf16x8 tr_frag(LAS char* p0, int off) {
  s16x4 lo = __builtin_amdgcn_ds_read_tr16_b64_v4i16((LAS s16x4*)(p0 + off));
  s16x4 hi = __builtin_amdgcn_ds_read_tr16_b64_v4i16((LAS s16x4*)(p0 + off + K1_WGRP));
  return __builtin_shufflevector(lo, hi, 0, 1, 2, 3, 4, 5, 6, 7);
}

template <bool KV>
DI void k1_compute(const u16* __restrict__ b1, char* __restrict__ ws, LAS char* smem, int rb, int cb, int w) {
  const int lane = threadIdx.x & 63;
  const int wr = w >> 1, wc = w & 1;
  const int g = lane >> 4, l15 = lane & 15;

  f32x4 acc[7][4];
#pragma unroll
  for (int t = 0; t < 7; ++t) {
    f32x4 init = {0.f, 0.f, 0.f, 0.f};
    if (!KV) {
      int T = 14 * (cb - 9) + 7 * wc + t;
      T = T < 0 ? 0 : (T > 95 ? 95 : T);
      const u32x2 bb = *(const u32x2*)(b1 + 16 * T + 4 * g);
      if (cb > 8) init = (f32x4){bflo(bb[0]), bfhi(bb[0]), bflo(bb[1]), bfhi(bb[1])};
    }
#pragma unroll
    for (int m = 0; m < 4; ++m) acc[t][m] = init;
  }
  const int xrd = (wr * 64 + l15) * 128;
  const int xsw = l15 & 7;
  const int wrow = K1_XB + (4 * (g & 1) + (l15 >> 2)) * K1_WBLK + (g >> 1) * 512;
  const int ka = 32 * (cb & 1);
  const int wrd = wrow + (KV ? ka + wc * 80 : wc * 224) + (l15 & 3) * 8;
  const int wrdq = wrow + 384 + wc * 64 + (l15 & 3) * 8;
  const int wrd4 = wrow + ka + wc * 80 + 32 + (l15 & 1) * 40 + ((l15 >> 1) & 1) * 192;

  bf16x8 a0[4], b0[7], a1[4], b1f[7];
  auto rd = [&](LAS char* sb, int s, bf16x8 (&a)[4], bf16x8 (&b)[7]) {
#pragma unroll
    for (int m = 0; m < 4; ++m) a[m] = *(LAS bf16x8*)(sb + xrd + m * 2048 + (((4 * s + g) ^ xsw) << 4));
    if (KV) {
      b[0] = tr_frag(sb + wrd, s * 2 * K1_WGRP + 0);
      b[1] = tr_frag(sb + wrd, s * 2 * K1_WGRP + 40);
      b[2] = tr_frag(sb + wrd, s * 2 * K1_WGRP + 192);
      b[3] = tr_frag(sb + wrd, s * 2 * K1_WGRP + 232);
      b[4] = tr_frag(sb + wrd4, s * 2 * K1_WGRP);
      b[5] = tr_frag(sb + wrdq, s * 2 * K1_WGRP);
      b[6] = tr_frag(sb + wrdq, s * 2 * K1_WGRP + 32);
    } else {
#pragma unroll
      for (int t = 0; t < 7; ++t) b[t] = tr_frag(sb + wrd, s * 2 * K1_WGRP + 32 * t);
    }
  };
  auto mm = [&](bf16x8 (&a)[4], bf16x8 (&b)[7]) {
#pragma unroll
    for (int t = 0; t < 7; ++t)
#pragma unroll
      for (int m = 0; m < 4; ++m) {
        if (KV && t < 5) acc[t][m] = MFMA16(a[m], b[t], acc[t][m]);
        else acc[t][m] = MFMA16(b[t], a[m], acc[t][m]);
      }
  };
  __builtin_amdgcn_s_barrier();
  asm volatile("" ::: "memory");
  rd(smem, 0, a0, b0);
  rd(smem, 1, a1, b1f);
  mm(a0, b0);
  int buf = 1;
#pragma unroll 1
  for (int i = 1; i < 10; ++i) {
    asm volatile("s_waitcnt lgkmcnt(0)" ::: "memory");
    __builtin_amdgcn_s_barrier();
    asm volatile("" ::: "memory");
    LAS char* sb = smem + buf * K1_STAGE;
    SCHED();
    rd(sb, 0, a0, b0);
    mm(a1, b1f);
    INTERLEAVE();
    SCHED();
    rd(sb, 1, a1, b1f);
    mm(a0, b0);
    INTERLEAVE();
    SCHED();
    buf = buf + 1; if (buf == 3) buf = 0;
  }
  mm(a1, b1f);
  STAMP(0, 3);

  char* afrag = ws + WS_AFRAG;
  const int rt0 = rb * 8 + wr * 4;
  if (KV) {
    const int hp = 2 * cb + wc;
    f32x4 R[10];
#pragma unroll
    for (int t = 0; t < 10; ++t) R[t] = (f32x4){0.f, 0.f, 0.f, 0.f};
    const bool iskl = l15 < 8;
    const bf16x8 ones = {0x3f80, 0x3f80, 0x3f80, 0x3f80, 0x3f80, 0x3f80, 0x3f80, 0x3f80};
#pragma unroll
    for (int u = 0; u < 2; ++u) {
      bf16x8 T[5];
#pragma unroll
      for (int t = 0; t < 5; ++t) {
        u32x4 p;
#pragma unroll
        for (int j = 0; j < 4; ++j) {
          const float e0 = acc[t][2 * u + (j >> 1)][2 * (j & 1)], e1 = acc[t][2 * u + (j >> 1)][2 * (j & 1) + 1];
          if (t < 2) p[j] = phi2(e0, e1);
          else if (t < 4) p[j] = pack2(e0, e1);
          else p[j] = iskl ? phi2(e0, e1) : pack2(e0, e1);
        }
        T[t] = __builtin_bit_cast(bf16x8, p);
      }
      R[0] = MFMA16(T[0], T[2], R[0]);
      R[1] = MFMA16(T[0], T[4], R[1]);
      R[2] = MFMA16(T[4], T[2], R[2]);
      R[3] = MFMA16(T[1], T[3], R[3]);
      R[4] = MFMA16(T[1], T[4], R[4]);
      R[5] = MFMA16(T[4], T[3], R[5]);
      R[6] = MFMA16(T[4], T[4], R[6]);
      R[7] = MFMA16(T[0], ones, R[7]);
      R[8] = MFMA16(T[1], ones, R[8]);
      R[9] = MFMA16(T[4], ones, R[9]);
    }
    LAS char* red = smem + K1_STAGE + wc * 10240;
    if (wr == 1) {
#pragma unroll
      for (int t = 0; t < 10; ++t) *(LAS f32x4*)(red + t * 1024 + lane * 16) = R[t];
    }
#pragma unroll
    for (int m = 0; m < 4; ++m) {
      u32x4 v;
      v[0] = phi2(acc[5][m][0], acc[5][m][1]);
      v[1] = phi2(acc[5][m][2], acc[5][m][3]);
      v[2] = phi2(acc[6][m][0], acc[6][m][1]);
      v[3] = phi2(acc[6][m][2], acc[6][m][3]);
      ST16(afrag + ((size_t)(rt0 + m) * NKS + 48 + hp) * 1024 + lane * 16, v);
    }
    asm volatile("s_waitcnt lgkmcnt(0)" ::: "memory");
    __builtin_amdgcn_s_barrier();
    asm volatile("" ::: "memory");
    if (wr == 0) {
#pragma unroll
      for (int t = 0; t < 10; ++t) R[t] += *(LAS f32x4*)(red + t * 1024 + lane * 16);
      char* kp = ws + WS_KVP + (size_t)rb * 51200 + (size_t)(10 * hp) * 320;
      *(f32x4*)(kp + g * 320 + l15 * 16) = R[0];
      *(f32x4*)(kp + 1600 + g * 320 + l15 * 16) = R[3];
      if (l15 >= 8) *(f32x4*)(kp + (l15 >= 12 ? 1600 : 0) + g * 320 + (16 + (l15 & 3)) * 16) = l15 >= 12 ? R[4] : R[1];
      if (g < 2) *(f32x4*)(kp + g * 1600 + 4 * 320 + l15 * 16) = g ? R[5] : R[2];
      if (g < 2 && (l15 >> 2) == 2 + g) *(f32x4*)(kp + g * 1600 + 4 * 320 + (16 + (l15 & 3)) * 16) = R[6];
      char* zp = ws + WS_ZP + (size_t)rb * 2560 + (size_t)(10 * hp) * 16;
      if (l15 < 2 || (l15 == 2 && g < 2))
        *(f32x4*)(zp + (l15 == 0 ? g * 16 : (l15 == 1 ? 80 + g * 16 : g * 80 + 64))) = l15 == 0 ? R[7] : (l15 == 1 ? R[8] : R[9]);
    }
  } else {
    if (cb == 8) {
#pragma unroll
      for (int pp = 0; pp < 2; ++pp)
#pragma unroll
        for (int m = 0; m < 4; ++m) {
          u32x4 v;
          v[0] = phi2(acc[2 * pp][m][0], acc[2 * pp][m][1]);
          v[1] = phi2(acc[2 * pp][m][2], acc[2 * pp][m][3]);
          v[2] = phi2(acc[2 * pp + 1][m][0], acc[2 * pp + 1][m][1]);
          v[3] = phi2(acc[2 * pp + 1][m][2], acc[2 * pp + 1][m][3]);
          ST16(afrag + ((size_t)(rt0 + m) * NKS + 48 + 16 + 2 * wc + pp) * 1024 + lane * 16, v);
        }
    } else {
      const int T0 = 14 * (cb - 9) + 7 * wc;
      u32x2 hv[7][4];
#pragma unroll
      for (int t = 0; t < 7; ++t) {
#pragma unroll
        for (int m = 0; m < 4; ++m) {
          hv[t][m][0] = sqrelu2(acc[t][m][0], acc[t][m][1]);
          hv[t][m][1] = sqrelu2(acc[t][m][2], acc[t][m][3]);
        }
      }
      char* base = afrag + ((size_t)rt0 * NKS + (T0 >> 1)) * 1024 + lane * 16;
      if (wc == 0) {
#pragma unroll
        for (int m = 0; m < 4; ++m) {
          char* bm = base + (size_t)m * NKS * 1024;
#pragma unroll
          for (int pp = 0; pp < 3; ++pp) {
            u32x4 v = {hv[2 * pp][m][0], hv[2 * pp][m][1], hv[2 * pp + 1][m][0], hv[2 * pp + 1][m][1]};
            ST16(bm + pp * 1024, v);
          }
          ST8(bm + 3 * 1024, hv[6][m]);
        }
      } else {
        const bool full = T0 + 6 < 96;
#pragma unroll
        for (int m = 0; m < 4; ++m) {
          char* bm = base + (size_t)m * NKS * 1024;
          ST8(bm + 8, hv[0][m]);
#pragma unroll
          for (int pp = 0; pp < 3; ++pp) {
            u32x4 v = {hv[2 * pp + 1][m][0], hv[2 * pp + 1][m][1], hv[2 * pp + 2][m][0], hv[2 * pp + 2][m][1]};
            if (pp < 2 || full) ST16(bm + (pp + 1) * 1024, v);
          }
        }
      }
    }
  }
}

__global__ void __launch_bounds__(512, 2)
k1_kernel(const u16* __restrict__ x, const u16* __restrict__ wqkv, const u16* __restrict__ w1,
          const u16* __restrict__ b1, const u16* __restrict__ w2, const u16* __restrict__ gate_w,
          char* __restrict__ ws) {
  extern __shared__ __attribute__((aligned(16))) char smem_[];
  LAS char* smem = (LAS char*)smem_;
  const int bid = blockIdx.x, tid = threadIdx.x, lane = tid & 63;
  const int w = __builtin_amdgcn_readfirstlane(tid >> 6);
  const int xcd = bid & 7, idx = bid >> 3;
  const int rb = 8 * (xcd >> 2) + (idx >> 2);
  const int cb = (xcd & 3) + 4 * (idx & 3);
  STAMP(0, 0);

  if (w < 4) {
    if (cb < 8) k1_compute<true>(b1, ws, smem, rb, cb, w);
    else k1_compute<false>(b1, ws, smem, rb, cb, w);
    STAMP(0, 4);
    return;
  }
  __builtin_amdgcn_s_setprio(3);
  const int lw = w - 4;
  const unsigned lds0 = (unsigned)(size_t)smem;
  const unsigned xoff = (unsigned)((lane >> 3) * (D_ * 2) + (((lane & 7) ^ (lane >> 3)) << 4));
  const char* xg = (const char*)x + (size_t)(rb * 128 + 8 * lw) * (D_ * 2);
  const int ldw = cb <= 8 ? 1920 : F_;
  const int rl = lane >> 5, c = lane & 31;
  bool wact;
  int col;
  if (cb < 8) {
    const int cs = c - 2 * (cb & 1);
    col = c >= 24 ? 64 * cb + 8 * (c - 24) : (cs < 12 ? 640 + 80 * cb + 8 * cs : 1280 + 80 * cb + 8 * (cs - 12));
    wact = c >= 24 || (cs >= 0 && cs < 10) || (cs >= 12 && cs < 22);
  } else if (cb == 8) {
    const int wcc = c >= 14 ? 1 : 0, cc = c - 14 * wcc;
    col = 512 + 64 * wcc + 8 * cc;
    wact = c < 28 && cc < 8;
  } else {
    col = 224 * (cb - 9) + 8 * c;
    wact = c < 28 && col < F_;
  }
  if (!wact) col = 0;
  const unsigned woff = (unsigned)((rl * 16 * ldw + col) * 2);
  const char* wg = (const char*)(cb <= 8 ? wqkv : w1) + (size_t)lw * ldw * 2;

  const int rot = ((idx >> 2) + ((5 * (idx & 3)) >> 1) + 3 * xcd) % 10;
  auto stage = [&](int it, int buf) {
    int kt = it + rot; if (kt >= 10) kt -= 10;
    const unsigned sb = lds0 + buf * K1_STAGE;
    const char* xs = xg + kt * 128;
#pragma unroll
    for (int j = 0; j < 4; ++j) dma16(sb + (lw + 4 * j) * 1024, xoff, xs + j * (32 * D_ * 2));
    const char* wsrc = wg + (size_t)kt * 64 * ldw * 2;
    if (wact) {
#pragma unroll
      for (int j = 0; j < 8; ++j) {
        const int kj = 32 * (j >> 2) + 8 * (j & 1) + 4 * ((j >> 1) & 1);
        dma16(sb + K1_XB + (lw + 4 * j) * K1_WBLK, woff, wsrc + (size_t)kj * ldw * 2);
      }
    }
  };
  stage(0, 0);
  stage(1, 1);
  STAMP(0, 1);
  ITS_DECL
  ITS(0, 0);
  asm volatile("s_waitcnt vmcnt(12)" ::: "memory");
  ITS(0, 1);
  __builtin_amdgcn_s_barrier();
  asm volatile("" ::: "memory");
  ITS(0, 2);
  stage(2, 2);
  ITS(0, 3);

  int nb = 0;
#pragma unroll 1
  for (int i = 1; i < 8; ++i) {
    ITS(i, 0);
    asm volatile("s_waitcnt vmcnt(12)" ::: "memory");
    ITS(i, 1);
    __builtin_amdgcn_s_barrier();
    asm volatile("" ::: "memory");
    ITS(i, 2);
    stage(i + 2, nb);
    ITS(i, 3);
    nb = nb + 1; if (nb == 3) nb = 0;
  }
  asm volatile("s_waitcnt vmcnt(12)" ::: "memory");
  __builtin_amdgcn_s_barrier();
  asm volatile("s_waitcnt vmcnt(0)" ::: "memory");
  __builtin_amdgcn_s_barrier();
  asm volatile("" ::: "memory");
  STAMP(0, 2);
  __builtin_amdgcn_s_setprio(0);
  const int u2 = 4 * bid + lw;
  const int ks2 = u2 / 20, np2 = u2 - 20 * ks2;
  u32x4 w2a = {0, 0, 0, 0}, w2b = {0, 0, 0, 0};
  if (u2 < 960) {
    const u16* src = w2 + (size_t)(32 * ks2 + (lane >> 2)) * D_ + 32 * np2 + 8 * (lane & 3);
    w2a = *(const u32x4*)src;
    w2b = *(const u32x4*)(src + 16 * D_);
  }
  const int grow = rb * 128 + 8 * cb + 2 * lw;
  u32x4 gx0 = *(const u32x4*)(x + (size_t)grow * D_ + 8 * lane);
  u32x4 gx1 = *(const u32x4*)(x + (size_t)(grow + 1) * D_ + 8 * lane);
  u32x4 gw0 = *(const u32x4*)(gate_w + 8 * lane);
  u32x4 gx2 = *(const u32x4*)(x + (size_t)(grow + ((lane >> 4) & 1)) * D_ + 512 + 8 * (lane & 15));
  u32x4 gw1 = *(const u32x4*)(gate_w + 512 + 8 * (lane & 15));
  {
    const u32x4 sent = {0xffffffffu, 0xffffffffu, 0xffffffffu, 0xffffffffu};
    if (u2 < 800) {
      const int nt = u2 / 20, ks = u2 - 20 * nt;
      *(u32x4*)(ws + WS_BFRAG + ((size_t)nt * NKS + 48 + ks) * 1024 + lane * 16) = sent;
    }
  }

  {
    float s0 = 0.f, s1 = 0.f, s2 = 0.f;
#pragma unroll
    for (int j = 0; j < 4; ++j) {
      s0 += rbf(bflo(gx0[j]) * bflo(gw0[j])) + rbf(bfhi(gx0[j]) * bfhi(gw0[j]));
      s1 += rbf(bflo(gx1[j]) * bflo(gw0[j])) + rbf(bfhi(gx1[j]) * bfhi(gw0[j]));
      s2 += rbf(bflo(gx2[j]) * bflo(gw1[j])) + rbf(bfhi(gx2[j]) * bfhi(gw1[j]));
    }
    if (lane < 16) s0 += s2;
    else if (lane < 32) s1 += s2;
#pragma unroll
    for (int o = 32; o >= 1; o >>= 1) {
      s0 += __shfl_xor(s0, o);
      s1 += __shfl_xor(s1, o);
    }
    if (lane < 2) {
      float l = rbf(lane == 0 ? s0 : s1);
      float gv = rbf(1.f / (1.f + __expf(-l)));
      ((float*)(ws + WS_G))[grow + lane] = gv;
    }
  }
  if (u2 < 960) {
    LAS char* t = smem + K1_SCR + lw * 3072;
    constexpr int RS = 96;
    *(LAS u32x4*)(t + (lane >> 2) * RS + (lane & 3) * 16) = w2a;
    *(LAS u32x4*)(t + (16 + (lane >> 2)) * RS + (lane & 3) * 16) = w2b;
    asm volatile("s_waitcnt lgkmcnt(0)" ::: "memory");
    const int g = lane >> 4, l15 = lane & 15;
#pragma unroll
    for (int i = 0; i < 2; ++i) {
      LAS char* p = t + (4 * g + (l15 >> 2)) * RS + i * 32 + (l15 & 3) * 8;
      s16x4 lo = __builtin_amdgcn_ds_read_tr16_b64_v4i16((LAS s16x4*)p);
      s16x4 hi = __builtin_amdgcn_ds_read_tr16_b64_v4i16((LAS s16x4*)(p + 16 * RS));
      bf16x8 v = __builtin_shufflevector(lo, hi, 0, 1, 2, 3, 4, 5, 6, 7);
      *(bf16x8*)(ws + WS_BFRAG + ((size_t)(2 * np2 + i) * NKS + ks2) * 1024 + lane * 16) = v;
    }
  }

  ITS_DUMP
  if (cb < 8) __builtin_amdgcn_s_barrier();
  STAMP(0, 4);
}

constexpr int K2_LDS = 8 * 20 * 1024;
#define WINF 3
constexpr int WIN = WINF;
#define MAUX 0

__global__ void __launch_bounds__(512, 2)
k2_kernel(const u16* __restrict__ wo, const u16* __restrict__ b2, char* ws, u16* __restrict__ out) {
  extern __shared__ __attribute__((aligned(16))) char smem_[];
  LAS char* smem = (LAS char*)smem_;
  const int bid = blockIdx.x, tid = threadIdx.x, lane = tid & 63;
  const int w = __builtin_amdgcn_readfirstlane(tid >> 6);
  const int g = lane >> 4, l15 = lane & 15;
  const int xcd = bid & 7, idx = bid >> 3;
  const int rb = 8 * (xcd >> 1) + (idx >> 2);
  const int nu = 4 * (xcd & 1) + (idx & 3);
  const __amdgpu_buffer_rsrc_t wsr = __builtin_amdgcn_make_buffer_rsrc(ws, 0, (int)WS_END, 0x00020000);
  const unsigned lv = lane * 16;
  const unsigned ap = (unsigned)(WS_AFRAG + (size_t)(4 * rb) * NKS * 1024);
#define LDF(soff) __builtin_bit_cast(bf16x8, __builtin_amdgcn_raw_buffer_load_b128(wsr, lv, (soff), 0))
  STAMP(1, 0);
  if (w < 4) {
    f32x4 acc[4][5];
#pragma unroll
    for (int m = 0; m < 4; ++m)
#pragma unroll
      for (int c = 0; c < 5; ++c) acc[m][c] = (f32x4){0.f, 0.f, 0.f, 0.f};
    bf16x8 A[WIN][4], B[WIN][5];
    const unsigned a0 = ap + 12 * w * 1024;
    const unsigned b0 = (unsigned)(WS_BFRAG + (size_t)(5 * nu) * NKS * 1024) + 12 * w * 1024;
#pragma unroll
    for (int s = 0; s < WIN; ++s) {
      const int so = s;
#pragma unroll
      for (int m = 0; m < 4; ++m) A[s][m] = LDF(a0 + (m * NKS + so) * 1024);
#pragma unroll
      for (int c = 0; c < 5; ++c) B[s][c] = LDF(b0 + (c * NKS + so) * 1024);
    }
    SCHED();
#pragma unroll
    for (int s = 0; s < 12; ++s) {
#pragma unroll
      for (int m = 0; m < 4; ++m)
#pragma unroll
        for (int c = 0; c < 5; ++c) acc[m][c] = MFMA16(B[s % WIN][c], A[s % WIN][m], acc[m][c]);
      SCHED();
      if (s + WIN < 12) {
        const int so = s + WIN;
#pragma unroll
        for (int m = 0; m < 4; ++m) A[s % WIN][m] = LDF(a0 + (m * NKS + so) * 1024);
#pragma unroll
        for (int c = 0; c < 5; ++c) B[s % WIN][c] = LDF(b0 + (c * NKS + so) * 1024);
      }
      SCHED();
    }
    STAMP(1, 2);
    LAS char* p = smem + w * 20480 + lane * 16;
#pragma unroll
    for (int m = 0; m < 4; ++m)
#pragma unroll
      for (int c = 0; c < 5; ++c) *(LAS f32x4*)(p + (m * 5 + c) * 1024) = acc[m][c];
  } else {
    __builtin_amdgcn_s_setprio(3);
    const int aw = w - 4;
    const int J = 32 * aw + idx;
    const int ksj = xcd + 8 * (J / 40);
    const bool job = J < 120 && ksj < 20;
    bf16x8 P[5][4];
    const unsigned a0 = ap + (48 + 5 * aw) * 1024;
    f32x4 zq[16];
    const unsigned zo = (unsigned)((40 * aw + (lane < 40 ? lane : 39)) * 16);
#define LOADZP() _Pragma("unroll") for (int r = 0; r < 16; ++r) \
      zq[r] = __builtin_bit_cast(f32x4, __builtin_amdgcn_raw_buffer_load_b128(wsr, zo, (unsigned)(WS_ZP + (size_t)r * 2560), 0))
    if (job) {
      const int ks = ksj;
      const int rem = J % 40, gg = rem / 10, ct = rem - 10 * gg;
      const int hf = lane >> 5, l31 = lane & 31;
      const int m = 8 * ks + 4 * hf + gg, ph = m / 5, dq = m - 5 * ph;
      LAS float* skv = (LAS float*)(smem + w * 20480);
      const unsigned kvo = (unsigned)(m * 320 + (l31 < 20 ? l31 : 19) * 16);
      f32x4 t[16];
#pragma unroll
      for (int r = 0; r < 16; ++r)
        t[r] = __builtin_bit_cast(f32x4, __builtin_amdgcn_raw_buffer_load_b128(wsr, kvo, (unsigned)(WS_KVP + (size_t)r * 51200), 0));
      unsigned wv[20];
#pragma unroll
      for (int e = 0; e < 20; ++e) wv[e] = *(const unsigned*)(wo + (size_t)(20 * ph + e) * D_ + 64 * ct + 2 * l31);
      SCHED();
      LOADZP();
#pragma unroll
      for (int tt = 0; tt < 5; ++tt)
#pragma unroll
        for (int mm = 0; mm < 4; ++mm) P[tt][mm] = LDF(a0 + (mm * NKS + tt) * 1024);
      SCHED();
      const f32x4 s = (((t[0] + t[1]) + (t[2] + t[3])) + ((t[4] + t[5]) + (t[6] + t[7]))) +
                      (((t[8] + t[9]) + (t[10] + t[11])) + ((t[12] + t[13]) + (t[14] + t[15])));
      if (l31 < 20) {
        const unsigned q0 = pack2(s[0], s[1]), q1 = pack2(s[2], s[3]);
        *(LAS f32x4*)(skv + hf * 80 + l31 * 4) = (f32x4){bflo(q0), bfhi(q0), bflo(q1), bfhi(q1)};
      }
      asm volatile("s_waitcnt lgkmcnt(0)" ::: "memory");
      f32x4 o0 = {0.f, 0.f, 0.f, 0.f}, o1 = {0.f, 0.f, 0.f, 0.f};
#pragma unroll
      for (int e = 0; e < 20; ++e) {
        f32x4 kvv = *(LAS f32x4*)(skv + hf * 80 + e * 4);
        o0 += kvv * bflo(wv[e]);
        o1 += kvv * bfhi(wv[e]);
      }
      const auto rx = __builtin_amdgcn_permlane32_swap(pack2(o0[0], o0[1]), pack2(o1[0], o1[1]), false, false);
      const auto ry = __builtin_amdgcn_permlane32_swap(pack2(o0[2], o0[3]), pack2(o1[2], o1[3]), false, false);
      const u32x4 v = {rx[0], ry[0], rx[1], ry[1]};
      const int n = 64 * ct + 2 * l31 + hf;
      __builtin_amdgcn_raw_buffer_store_b128(v, wsr, (unsigned)(WS_BFRAG + ((size_t)(n >> 4) * NKS + 48 + ks) * 1024 + (16 * gg + (n & 15)) * 16), 0, 16);
    } else {
      LOADZP();
#pragma unroll
      for (int tt = 0; tt < 5; ++tt)
#pragma unroll
        for (int mm = 0; mm < 4; ++mm) P[tt][mm] = LDF(a0 + (mm * NKS + tt) * 1024);
    }
    SCHED();
    __builtin_amdgcn_s_setprio(0);
    STAMP(1, 1);
    u32x4 Mf[3][5];
    const unsigned mo = (unsigned)(WS_BFRAG + ((size_t)(5 * nu) * NKS + 48 + 5 * aw) * 1024);
    u32x4 Z[5];
    {
      const f32x4 zs = (((zq[0] + zq[1]) + (zq[2] + zq[3])) + ((zq[4] + zq[5]) + (zq[6] + zq[7]))) +
                       (((zq[8] + zq[9]) + (zq[10] + zq[11])) + ((zq[12] + zq[13]) + (zq[14] + zq[15])));
      LAS char* zl = smem + w * 20480 + 1024;
      if (lane < 40) *(LAS u32x2*)(zl + (((lane >> 3) * 4 + (lane & 3)) * 2 + ((lane >> 2) & 1)) * 8) = (u32x2){pack2(zs[0], zs[1]), pack2(zs[2], zs[3])};
      asm volatile("s_waitcnt lgkmcnt(0)" ::: "memory");
#pragma unroll
      for (int t = 0; t < 5; ++t) Z[t] = *(LAS u32x4*)(zl + (t * 4 + g) * 16);
    }
    STAMP(1, 2);
    {
      f32x4 Dn[4][3];
#pragma unroll
      for (int sg = 0; sg < 3; ++sg) {
        const int hr = ((l15 >> 2) + 4 * (4 * sg + (l15 & 3))) / 5;
#pragma unroll
        for (int m = 0; m < 4; ++m) Dn[m][sg] = (f32x4){0.f, 0.f, 0.f, 0.f};
#pragma unroll
        for (int t = 0; t < 5; ++t) {
          const bool used = (sg == 0 && t <= 2) || (sg == 1 && t >= 1) || (sg == 2 && t >= 3);
          if (used) {
            const bool k0 = ((8 * t + g) / 5) == hr, k1 = ((8 * t + 4 + g) / 5) == hr;
            u32x4 v = {k0 ? Z[t][0] : 0u, k0 ? Z[t][1] : 0u, k1 ? Z[t][2] : 0u, k1 ? Z[t][3] : 0u};
            const bf16x8 zm = __builtin_bit_cast(bf16x8, v);
#pragma unroll
            for (int m = 0; m < 4; ++m) Dn[m][sg] = MFMA16(zm, P[t][m], Dn[m][sg]);
          }
        }
      }
#pragma unroll
      for (int m = 0; m < 4; ++m) {
#pragma unroll
        for (int t = 0; t < 5; ++t) {
          u32x4 p = __builtin_bit_cast(u32x4, P[t][m]);
          u32x4 q;
          const unsigned d1 = pack2(Dn[m][(2 * t) >> 2][(2 * t) & 3], Dn[m][(2 * t + 1) >> 2][(2 * t + 1) & 3]);
          const unsigned d2 = pack2(bflo(d1) + 1e-6f, bfhi(d1) + 1e-6f);
          const float r0 = __builtin_amdgcn_rcpf(bflo(d2)), r1 = __builtin_amdgcn_rcpf(bfhi(d2));
          q[0] = pack2(bflo(p[0]) * r0, bfhi(p[0]) * r0);
          q[1] = pack2(bflo(p[1]) * r0, bfhi(p[1]) * r0);
          q[2] = pack2(bflo(p[2]) * r1, bfhi(p[2]) * r1);
          q[3] = pack2(bflo(p[3]) * r1, bfhi(p[3]) * r1);
          P[t][m] = __builtin_bit_cast(bf16x8, q);
          asm volatile("" : "+v"(P[t][m]));
        }
      }
    }
    SCHED();
    STAMP(1, 6);
    {
      const int pl = lane < 40 ? lane : 39, pt = pl >> 3, pg = (pl >> 1) & 3, pe = pl & 1;
      const unsigned po = mo + ((pe ? 4 : 0) * NKS + pt) * 1024 + (16 * pg + (pe ? 15 : 0)) * 16;
      for (unsigned spins = 0;; ++spins) {
        const unsigned v = __builtin_amdgcn_raw_buffer_load_b32(wsr, po, 0, 16);
        if (__all(v != 0xffffffffu) || spins > (1u << 16)) break;
        __builtin_amdgcn_s_sleep(4);
      }
    }
    STAMP(1, 7);
    __builtin_amdgcn_s_setprio(2);
#pragma unroll
    for (int t = 0; t < 3; ++t)
#pragma unroll
      for (int c = 0; c < 5; ++c) Mf[t][c] = __builtin_amdgcn_raw_buffer_load_b128(wsr, lv, mo + (c * NKS + t) * 1024, 16);
    SCHED();
    f32x4 acc[4][5];
    for (unsigned spins = 0;; ++spins) {
      unsigned mx = 0u;
#pragma unroll
      for (int t = 0; t < 3; ++t)
#pragma unroll
        for (int c = 0; c < 5; ++c) mx = max(max(mx, max(Mf[t][c][0], Mf[t][c][1])), max(Mf[t][c][2], Mf[t][c][3]));
      if (__builtin_expect(__all(mx != 0xffffffffu) || spins > (1u << 16), 1)) break;
      __builtin_amdgcn_s_sleep(8);
#pragma unroll
      for (int t = 0; t < 3; ++t)
#pragma unroll
        for (int c = 0; c < 5; ++c) Mf[t][c] = __builtin_amdgcn_raw_buffer_load_b128(wsr, lv, mo + (c * NKS + t) * 1024, 16);
    }
#pragma unroll
    for (int t = 0; t < 3; ++t) {
#pragma unroll
      for (int m = 0; m < 4; ++m)
#pragma unroll
        for (int c = 0; c < 5; ++c)
          acc[m][c] = MFMA16(__builtin_bit_cast(bf16x8, Mf[t][c]), P[t][m], t == 0 ? ((f32x4){0.f, 0.f, 0.f, 0.f}) : acc[m][c]);
      SCHED();
      if (t < 2) {
#pragma unroll
        for (int c = 0; c < 5; ++c) Mf[t][c] = __builtin_amdgcn_raw_buffer_load_b128(wsr, lv, mo + (c * NKS + t + 3) * 1024, 16);
      }
      SCHED();
    }
    for (unsigned spins = 0;; ++spins) {
      unsigned mx = 0u;
#pragma unroll
      for (int t = 0; t < 2; ++t)
#pragma unroll
        for (int c = 0; c < 5; ++c) mx = max(max(mx, max(Mf[t][c][0], Mf[t][c][1])), max(Mf[t][c][2], Mf[t][c][3]));
      if (__builtin_expect(__all(mx != 0xffffffffu) || spins > (1u << 16), 1)) break;
      __builtin_amdgcn_s_sleep(8);
#pragma unroll
      for (int t = 0; t < 2; ++t)
#pragma unroll
        for (int c = 0; c < 5; ++c) Mf[t][c] = __builtin_amdgcn_raw_buffer_load_b128(wsr, lv, mo + (c * NKS + t + 3) * 1024, 16);
    }
#pragma unroll
    for (int t = 3; t < 5; ++t) {
#pragma unroll
      for (int m = 0; m < 4; ++m)
#pragma unroll
        for (int c = 0; c < 5; ++c) acc[m][c] = MFMA16(__builtin_bit_cast(bf16x8, Mf[t - 3][c]), P[t][m], acc[m][c]);
    }
    STAMP(1, 3);
    LAS char* p = smem + w * 20480 + lane * 16;
#pragma unroll
    for (int m = 0; m < 4; ++m)
#pragma unroll
      for (int c = 0; c < 5; ++c) *(LAS f32x4*)(p + (m * 5 + c) * 1024) = acc[m][c];
  }
  const int fm = w >> 1, c0 = (w & 1) ? 3 : 0, nc = (w & 1) ? 2 : 3;
  const int row = 64 * rb + 16 * fm + l15;
  const float gv = ((const float*)(ws + WS_G))[row];
  u32x2 bb[3];
#pragma unroll
  for (int i = 0; i < 3; ++i) bb[i] = *(const u32x2*)(b2 + 80 * nu + 16 * (c0 + (i < nc ? i : 0)) + 4 * g);
  __syncthreads();
  STAMP(1, 4);
  {
    const float om = rbf(1.f - gv);
#pragma unroll
    for (int i = 0; i < 3; ++i) {
      if (i < nc) {
        const int c = c0 + i;
        LAS char* p = smem + (fm * 5 + c) * 1024 + lane * 16;
        f32x4 fb = (*(LAS f32x4*)(p) + *(LAS f32x4*)(p + 20480)) + (*(LAS f32x4*)(p + 2 * 20480) + *(LAS f32x4*)(p + 3 * 20480));
        f32x4 fa = (*(LAS f32x4*)(p + 4 * 20480) + *(LAS f32x4*)(p + 5 * 20480)) + (*(LAS f32x4*)(p + 6 * 20480) + *(LAS f32x4*)(p + 7 * 20480));
        const int n = 80 * nu + 16 * c + 4 * g;
        const float bv[4] = {bflo(bb[i][0]), bfhi(bb[i][0]), bflo(bb[i][1]), bfhi(bb[i][1])};
        float e[4];
#pragma unroll
        for (int r = 0; r < 4; ++r) {
          float bfv = rbf(rbf(fb[r]) + bv[r]);
          float ga = rbf(gv * rbf(fa[r]));
          float gb = rbf(om * bfv);
          e[r] = ga + gb;
        }
        u32x2 v = {pack2(e[0], e[1]), pack2(e[2], e[3])};
        *(u32x2*)(out + (size_t)row * D_ + n) = v;
      }
    }
  }
  STAMP(1, 5);
}

extern "C" void kernel_launch(void* const* d_in, const int* in_sizes, int n_in, void* d_out, int out_size,
                              void* d_ws, size_t ws_size, hipStream_t stream) {
  const u16* x = (const u16*)d_in[0];
  const u16* wqkv = (const u16*)d_in[1];
  const u16* wo = (const u16*)d_in[2];
  const u16* w1 = (const u16*)d_in[4];
  const u16* b1 = (const u16*)d_in[5];
  const u16* w2 = (const u16*)d_in[6];
  const u16* b2 = (const u16*)d_in[7];
  const u16* gw = (const u16*)d_in[8];
  char* ws = (char*)d_ws;
  (void)hipFuncSetAttribute((const void*)k1_kernel, hipFuncAttributeMaxDynamicSharedMemorySize, K1_LDS);
  (void)hipFuncSetAttribute((const void*)k2_kernel, hipFuncAttributeMaxDynamicSharedMemorySize, K2_LDS);
  k1_kernel<<<256, 512, K1_LDS, stream>>>(x, wqkv, w1, b1, w2, gw, ws);
  k2_kernel<<<256, 512, K2_LDS, stream>>>(wo, b2, ws, (u16*)d_out);
}
```

```cpp
#include <hip/hip_runtime.h>
#include <hip/hip_bf16.h>
#include <stdint.h>

typedef __attribute__((ext_vector_type(8))) short bf16x8;
typedef __attribute__((ext_vector_type(4))) short s16x4;
typedef __attribute__((ext_vector_type(2))) short s16x2;
typedef __attribute__((ext_vector_type(4))) float f32x4;
typedef __attribute__((ext_vector_type(2))) float f32x2;
typedef __attribute__((ext_vector_type(2))) __bf16 bf16x2v;
typedef __attribute__((ext_vector_type(4))) unsigned u32x4;
typedef __attribute__((ext_vector_type(2))) unsigned u32x2;
typedef unsigned short u16;
typedef unsigned long long u64;
#define LAS __attribute__((address_space(3)))
#define GAS __attribute__((address_space(1)))
#define DI __device__ __forceinline__

constexpr int S_ = 2048, D_ = 640, F_ = 1536;
constexpr int NKS = 68;

constexpr unsigned WS_EPOCH = 0;
constexpr unsigned WS_F1 = 1024;
constexpr unsigned WS_FG = 8192;
constexpr unsigned WS_F0 = 4096;
constexpr unsigned WS_AFRAG = 16384;
constexpr unsigned WS_MFRAG = WS_AFRAG + 128 * NKS * 1024;
constexpr unsigned WS_W2F = WS_MFRAG + 40 * 20 * 1024;
constexpr unsigned WS_KVP = WS_W2F + 40 * 48 * 1024;
constexpr unsigned WS_ZP = WS_KVP + 16 * 51200;
constexpr unsigned WS_G = WS_ZP + 16 * 2560;
constexpr unsigned WS_END = WS_G + 2048 * 4;

DI unsigned pack2(float a, float b) {
  f32x2 f = {a, b};
  return __builtin_bit_cast(unsigned, __builtin_convertvector(f, bf16x2v));
}
DI float rbf(float a) { return __uint_as_float(pack2(a, a) << 16); }
DI float bflo(unsigned u) { return __uint_as_float(u << 16); }
DI float bfhi(unsigned u) { return __uint_as_float(u & 0xffff0000u); }
DI unsigned phi2(float a0, float a1) {
  const unsigned p = pack2(a0, a1);
  const float t0 = bflo(p), t1 = bfhi(p);
  const float e0 = __builtin_amdgcn_exp2f(fminf(t0 * 1.44269504f, 0.f));
  const float e1 = __builtin_amdgcn_exp2f(fminf(t1 * 1.44269504f, 0.f));
  return pack2((fmaxf(t0, 0.f) + e0) + 1e-6f, (fmaxf(t1, 0.f) + e1) + 1e-6f);
}
DI f32x4 sel4(bool c, f32x4 a, f32x4 b) {
  f32x4 r;
#pragma unroll
  for (int i = 0; i < 4; ++i) { float x = a[i], y = b[i]; asm volatile("" : "+v"(x), "+v"(y)); r[i] = c ? x : y; }
  return r;
}
DI unsigned sqrelu2(float a0, float a1) {
  unsigned q = pack2(a0, a1);
  const s16x2 z = {0, 0};
  q = __builtin_bit_cast(unsigned, __builtin_elementwise_max(__builtin_bit_cast(s16x2, q), z));
  const float u0 = bflo(q), u1 = bfhi(q);
  return pack2(u0 * u0, u1 * u1);
}
#define STAMP(kern,i)
#define MFMA16(a, b, c) __builtin_amdgcn_mfma_f32_16x16x32_bf16((a), (b), (c), 0, 0, 0)
#define SCHED() __builtin_amdgcn_sched_barrier(0)
#define SGB(mask, n) __builtin_amdgcn_sched_group_barrier(mask, n, 0)
#define INTERLEAVE() do { _Pragma("unroll") for (int q_ = 0; q_ < 18; ++q_) { SGB(0x100, 1); SGB(0x8, 1); } SGB(0x8, 10); } while (0)
#define BAR() do { asm volatile("s_waitcnt lgkmcnt(0)" ::: "memory"); __builtin_amdgcn_s_barrier(); asm volatile("" ::: "memory"); } while (0)
#define DRAIN() asm volatile("s_waitcnt vmcnt(0)" ::: "memory")
#define ST16(off, v) __builtin_amdgcn_raw_buffer_store_b128(__builtin_bit_cast(u32x4, v), wsr, (unsigned)(off), 0, 16)
#define ST8(off, v) __builtin_amdgcn_raw_buffer_store_b64((v), wsr, (unsigned)(off), 0, 16)
#define LD16(voff, soff) __builtin_amdgcn_raw_buffer_load_b128(wsr, (unsigned)(voff), (unsigned)(soff), 16)
constexpr unsigned SPIN_CAP = 1u << 18;
#define TOUCH_AT 10
#define DO_TOUCH (TOUCH_AT < 10)
#define POLL_SLEEP 8

constexpr int K1_XB = 16384;
constexpr int K1_WBLK = 1056;
constexpr int K1_WGRP = 8 * K1_WBLK;
constexpr int K1_WB = 32 * K1_WBLK;
constexpr int K1_STAGE = K1_XB + K1_WB;
constexpr int RING1 = 81920;
constexpr int KVRED = 61440;
constexpr int FSCR = 143360;
constexpr int ASCR = 150528;
constexpr int CTRL = 162816;
constexpr int LDS_BYTES = 163840;

DI void dma16(unsigned lds_off, unsigned voff, const char* gbase) {
  const unsigned lo = __builtin_amdgcn_readfirstlane((unsigned)(uintptr_t)gbase);
  const unsigned hi = __builtin_amdgcn_readfirstlane((unsigned)((uintptr_t)gbase >> 32));
  const u64 gb = ((u64)hi << 32) | lo;
  const unsigned lo2 = __builtin_amdgcn_readfirstlane(lds_off);
  asm volatile("s_mov_b32 m0, %0\n\ts_nop 3\n\tglobal_load_lds_dwordx4 %1, %2"
               :: "s"(lo2), "v"(voff), "s"(gb) : "memory");
}

DI void dma16_sc1(unsigned lds_off, unsigned voff, const char* gbase) {
  const unsigned lo = __builtin_amdgcn_readfirstlane((unsigned)(uintptr_t)gbase);
  const unsigned hi = __builtin_amdgcn_readfirstlane((unsigned)((uintptr_t)gbase >> 32));
  const u64 gb = ((u64)hi << 32) | lo;
  const unsigned lo2 = __builtin_amdgcn_readfirstlane(lds_off);
  asm volatile("s_mov_b32 m0, %0\n\ts_nop 3\n\tglobal_load_lds_dwordx4 %1, %2 sc1"
               :: "s"(lo2), "v"(voff), "s"(gb) : "memory");
}

DI u64 load_tag(const char* ws) {
  u64 e;
  asm volatile("s_load_dwordx2 %0, %1, 0x0\n\ts_waitcnt lgkmcnt(0)" : "=s"(e) : "s"(ws) : "memory");
  return e + 1ull;
}
DI bf16x8 tr_frag(LAS char* p0, int off, int step) {
  s16x4 lo = __builtin_amdgcn_ds_read_tr16_b64_v4i16((LAS s16x4*)(p0 + off));
  s16x4 hi = __builtin_amdgcn_ds_read_tr16_b64_v4i16((LAS s16x4*)(p0 + off + step));
  return __builtin_shufflevector(lo, hi, 0, 1, 2, 3, 4, 5, 6, 7);
}

DI void wait_flags(char* ws, unsigned off, bool care, u64 tag) {
  GAS u64* p = (GAS u64*)(ws + off);
  for (unsigned spins = 0;; ++spins) {
    const u64 v = __hip_atomic_load(p, __ATOMIC_RELAXED, __HIP_MEMORY_SCOPE_AGENT);
    if (__all(!care || v == tag) || spins > SPIN_CAP) break;
    __builtin_amdgcn_s_sleep(POLL_SLEEP);
  }
  __builtin_amdgcn_fence(__ATOMIC_ACQUIRE, "wavefront");
  asm volatile("" ::: "memory");
}

DI void lds_signal(LAS char* smem, int word, int lane) {
  asm volatile("s_waitcnt lgkmcnt(0)" ::: "memory");
  if (lane == 0) __hip_atomic_fetch_add((LAS unsigned*)(smem + CTRL) + word, 1u, __ATOMIC_RELAXED, __HIP_MEMORY_SCOPE_WORKGROUP);
}
DI void lds_wait(LAS char* smem, int word, unsigned n) {
  while (__builtin_amdgcn_readfirstlane(__hip_atomic_load((LAS unsigned*)(smem + CTRL) + word, __ATOMIC_RELAXED, __HIP_MEMORY_SCOPE_WORKGROUP)) < n)
    __builtin_amdgcn_s_sleep(1);
  asm volatile("" ::: "memory");
}
DI void arrive_publish(LAS char* smem, int word, char* ws, unsigned flag_off, u64 tag, unsigned n, int lane) {
  if (lane == 0) {
    const unsigned old = __hip_atomic_fetch_add((LAS unsigned*)(smem + CTRL) + word, 1u, __ATOMIC_RELAXED, __HIP_MEMORY_SCOPE_WORKGROUP);
    if (old == n - 1) {
      __hip_atomic_store((GAS u64*)(ws + flag_off), tag, __ATOMIC_RELAXED, __HIP_MEMORY_SCOPE_AGENT);
    }
  }
}

template <bool KV>
DI void p1_compute(const u32x2 (&bb)[7], char* ws, __amdgpu_buffer_rsrc_t wsr, LAS char* smem, int rb, int cb, int w, u64 tag) {
  const int lane = threadIdx.x & 63;
  const int wr = w >> 1, wc = w & 1;
  const int g = lane >> 4, l15 = lane & 15;

  const int xrd = (wr * 64 + l15) * 128;
  const int xsw = l15 & 7;
  const int wrow = K1_XB + (4 * (g & 1) + (l15 >> 2)) * K1_WBLK + (g >> 1) * 512;
  const int ka = 32 * (cb & 1);
  const int wrd = wrow + (KV ? ka + wc * 80 : wc * 224) + (l15 & 3) * 8;
  const int wrdq = wrow + 384 + wc * 64 + (l15 & 3) * 8;
  const int wrd4 = wrow + ka + wc * 80 + 32 + (l15 & 1) * 40 + ((l15 >> 1) & 1) * 192;

  f32x4 acc[7][4];
  bf16x8 a0[4], b0[7], a1[4], b1f[7];
  auto rd = [&](LAS char* sb, int s, bf16x8 (&a)[4], bf16x8 (&b)[7]) {
#pragma unroll
    for (int m = 0; m < 4; ++m) a[m] = *(LAS bf16x8*)(sb + xrd + m * 2048 + (((4 * s + g) ^ xsw) << 4));
    if (KV) {
      b[0] = tr_frag(sb + wrd, s * 2 * K1_WGRP + 0, K1_WGRP);
      b[1] = tr_frag(sb + wrd, s * 2 * K1_WGRP + 40, K1_WGRP);
      b[2] = tr_frag(sb + wrd, s * 2 * K1_WGRP + 192, K1_WGRP);
      b[3] = tr_frag(sb + wrd, s * 2 * K1_WGRP + 232, K1_WGRP);
      b[4] = tr_frag(sb + wrd4, s * 2 * K1_WGRP, K1_WGRP);
      b[5] = tr_frag(sb + wrdq, s * 2 * K1_WGRP, K1_WGRP);
      b[6] = tr_frag(sb + wrdq, s * 2 * K1_WGRP + 32, K1_WGRP);
    } else {
#pragma unroll
      for (int t = 0; t < 7; ++t) b[t] = tr_frag(sb + wrd, s * 2 * K1_WGRP + 32 * t, K1_WGRP);
    }
  };
  auto mm = [&](bf16x8 (&a)[4], bf16x8 (&b)[7]) {
#pragma unroll
    for (int t = 0; t < 7; ++t)
#pragma unroll
      for (int m = 0; m < 4; ++m) {
        if (KV && t < 5) acc[t][m] = MFMA16(a[m], b[t], acc[t][m]);
        else acc[t][m] = MFMA16(b[t], a[m], acc[t][m]);
      }
  };
  BAR();
  rd(smem, 0, a0, b0);
  rd(smem, 1, a1, b1f);

#pragma unroll
  for (int t = 0; t < 7; ++t) {
    f32x4 init = {0.f, 0.f, 0.f, 0.f};
    if (!KV && cb > 8) init = (f32x4){bflo(bb[t][0]), bfhi(bb[t][0]), bflo(bb[t][1]), bfhi(bb[t][1])};
#pragma unroll
    for (int m = 0; m < 4; ++m) acc[t][m] = init;
  }
  mm(a0, b0);
  int buf = 1;
  auto iter = [&]() {
    BAR();
    LAS char* sb = smem + buf * K1_STAGE;
    SCHED();
    rd(sb, 0, a0, b0);
    mm(a1, b1f);
    INTERLEAVE();
    SCHED();
    rd(sb, 1, a1, b1f);
    mm(a0, b0);
    INTERLEAVE();
    SCHED();
    buf = buf + 1; if (buf == 3) buf = 0;
  };
#pragma unroll 1
  for (int i = 1; i < TOUCH_AT; ++i) iter();
  unsigned t0_ = 0, t1_ = 0;
  if (DO_TOUCH && __builtin_amdgcn_readfirstlane(__hip_atomic_load((LAS unsigned*)(smem + CTRL) + 9, __ATOMIC_RELAXED, __HIP_MEMORY_SCOPE_WORKGROUP)) != 0u) {
    const unsigned ln = ((blockIdx.x >> 3) * 4 + w) * 64 + lane;
    t0_ = __builtin_amdgcn_raw_buffer_load_b32(wsr, WS_W2F + ln * 128, 0, 16);
    t1_ = __builtin_amdgcn_raw_buffer_load_b32(wsr, WS_W2F + (ln < 7168 ? ln + 8192 : ln) * 128, 0, 16);
  }
#pragma unroll 1
  for (int i = TOUCH_AT; i < 10; ++i) iter();
  asm volatile("" :: "v"(t0_), "v"(t1_));
  lds_signal(smem, 2, lane);
  __builtin_amdgcn_s_setprio(3);
  mm(a1, b1f);
  STAMP(0, 3);

  const int rt0 = rb * 8 + wr * 4;
  if (KV) {
    const int hp = 2 * cb + wc;
#pragma unroll
    for (int m = 0; m < 4; ++m) {
      u32x4 v;
      v[0] = phi2(acc[5][m][0], acc[5][m][1]);
      v[1] = phi2(acc[5][m][2], acc[5][m][3]);
      v[2] = phi2(acc[6][m][0], acc[6][m][1]);
      v[3] = phi2(acc[6][m][2], acc[6][m][3]);
      ST16(WS_AFRAG + ((rt0 + m) * NKS + 48 + hp) * 1024 + lane * 16, v);
    }
    SCHED();
    f32x4 R[10];
#pragma unroll
    for (int t = 0; t < 10; ++t) R[t] = (f32x4){0.f, 0.f, 0.f, 0.f};
    const bool iskl = l15 < 8;
    const bf16x8 ones = {0x3f80, 0x3f80, 0x3f80, 0x3f80, 0x3f80, 0x3f80, 0x3f80, 0x3f80};
#pragma unroll
    for (int u = 0; u < 2; ++u) {
      bf16x8 T[5];
#pragma unroll
      for (int t = 0; t < 5; ++t) {
        u32x4 p;
#pragma unroll
        for (int j = 0; j < 4; ++j) {
          const float e0 = acc[t][2 * u + (j >> 1)][2 * (j & 1)], e1 = acc[t][2 * u + (j >> 1)][2 * (j & 1) + 1];
          if (t < 2) p[j] = phi2(e0, e1);
          else if (t < 4) p[j] = pack2(e0, e1);
          else p[j] = iskl ? phi2(e0, e1) : pack2(e0, e1);
        }
        T[t] = __builtin_bit_cast(bf16x8, p);
      }
      R[0] = MFMA16(T[0], T[2], R[0]);
      R[1] = MFMA16(T[0], T[4], R[1]);
      R[2] = MFMA16(T[4], T[2], R[2]);
      R[3] = MFMA16(T[1], T[3], R[3]);
      R[4] = MFMA16(T[1], T[4], R[4]);
      R[5] = MFMA16(T[4], T[3], R[5]);
      R[6] = MFMA16(T[4], T[4], R[6]);
      R[7] = MFMA16(T[0], ones, R[7]);
      R[8] = MFMA16(T[1], ones, R[8]);
      R[9] = MFMA16(T[4], ones, R[9]);
    }
    LAS char* red = smem + KVRED + wc * 10240;
    if (wr == 1) {
#pragma unroll
      for (int t = 0; t < 10; ++t) *(LAS f32x4*)(red + t * 1024 + lane * 16) = R[t];
      lds_signal(smem, 3 + wc, lane);
    }
    lds_signal(smem, 7, lane);
    DRAIN();
    arrive_publish(smem, 0, ws, WS_F1 + (rb * 16 + cb) * 8, tag, 4, lane);
    if (wr == 0) {
      lds_wait(smem, 3 + wc, 1);
#pragma unroll
      for (int t = 0; t < 10; ++t) R[t] += *(LAS f32x4*)(red + t * 1024 + lane * 16);
      const unsigned kp = WS_KVP + rb * 51200 + (10 * hp) * 320;
      ST16(kp + g * 320 + l15 * 16, R[0]);
      ST16(kp + 1600 + g * 320 + l15 * 16, R[3]);
      if (l15 >= 8) ST16(kp + (l15 >= 12 ? 1600 : 0) + g * 320 + (16 + (l15 & 3)) * 16, sel4(l15 >= 12, R[4], R[1]));
      if (g < 2) ST16(kp + g * 1600 + 4 * 320 + l15 * 16, sel4(g != 0, R[5], R[2]));
      if (g < 2 && (l15 >> 2) == 2 + g) ST16(kp + g * 1600 + 4 * 320 + (16 + (l15 & 3)) * 16, R[6]);
      const unsigned zp = WS_ZP + rb * 2560 + (10 * hp) * 16;
      if (l15 < 2 || (l15 == 2 && g < 2))
        ST16(zp + (l15 == 0 ? g * 16 : (l15 == 1 ? 80 + g * 16 : g * 80 + 64)), sel4(l15 == 0, R[7], sel4(l15 == 1, R[8], R[9])));
    }
  } else {
    if (cb == 8) {
#pragma unroll
      for (int pp = 0; pp < 2; ++pp)
#pragma unroll
        for (int m = 0; m < 4; ++m) {
          u32x4 v;
          v[0] = phi2(acc[2 * pp][m][0], acc[2 * pp][m][1]);
          v[1] = phi2(acc[2 * pp][m][2], acc[2 * pp][m][3]);
          v[2] = phi2(acc[2 * pp + 1][m][0], acc[2 * pp + 1][m][1]);
          v[3] = phi2(acc[2 * pp + 1][m][2], acc[2 * pp + 1][m][3]);
          ST16(WS_AFRAG + ((rt0 + m) * NKS + 48 + 16 + 2 * wc + pp) * 1024 + lane * 16, v);
        }
    } else {
      const int T0 = 14 * (cb - 9) + 7 * wc;
      const unsigned base = WS_AFRAG + (rt0 * NKS + (T0 >> 1)) * 1024 + lane * 16;
      const bool full = T0 + 6 < 96;
#pragma unroll
      for (int m = 0; m < 4; ++m) {
        u32x2 hv[7];
#pragma unroll
        for (int t = 0; t < 7; ++t) {
          hv[t][0] = sqrelu2(acc[t][m][0], acc[t][m][1]);
          hv[t][1] = sqrelu2(acc[t][m][2], acc[t][m][3]);
        }
        const unsigned bm = base + m * NKS * 1024;
        if (wc == 0) {
#pragma unroll
          for (int pp = 0; pp < 3; ++pp) {
            u32x4 v = {hv[2 * pp][0], hv[2 * pp][1], hv[2 * pp + 1][0], hv[2 * pp + 1][1]};
            ST16(bm + pp * 1024, v);
          }
          ST8(bm + 3 * 1024, hv[6]);
        } else {
          ST8(bm + 8, hv[0]);
#pragma unroll
          for (int pp = 0; pp < 3; ++pp) {
            u32x4 v = {hv[2 * pp + 1][0], hv[2 * pp + 1][1], hv[2 * pp + 2][0], hv[2 * pp + 2][1]};
            if (pp < 2 || full) ST16(bm + (pp + 1) * 1024, v);
          }
        }
      }
    }
    lds_signal(smem, 7, lane);
    DRAIN();
    arrive_publish(smem, 0, ws, WS_F1 + (rb * 16 + cb) * 8, tag, 4, lane);
  }
}

#define WINF 4
constexpr int WIN = WINF;
static_assert(WIN <= 6, "ring fills must be older than the h loads of the same step");
constexpr int fops(int it) { return (it < 3 ? 5 : 0) + (it + WIN < 12 ? 4 : 0); }
constexpr int nwait(int s) {
  int n = 0;
  if (s < WIN) { n += 4 * (WIN - 1 - s); for (int it = 0; it < s; ++it) n += fops(it); }
  else { for (int it = s - WIN + 1; it < s; ++it) n += fops(it); }
  return n;
}
template <int N> DI void waitv() { asm volatile("s_waitcnt vmcnt(%0)" :: "n"(N) : "memory"); }

__global__ void __launch_bounds__(512, 2)
fused_kernel(const u16* __restrict__ x, const u16* __restrict__ wqkv, const u16* __restrict__ wo,
             const u16* __restrict__ w1, const u16* __restrict__ b1, const u16* __restrict__ w2,
             const u16* __restrict__ b2, const u16* __restrict__ gate_w, char* ws, u16* __restrict__ out) {
  extern __shared__ __attribute__((aligned(16))) char smem_[];
  LAS char* smem = (LAS char*)smem_;
  const int bid = blockIdx.x, tid = threadIdx.x, lane = tid & 63;
  const int w = __builtin_amdgcn_readfirstlane(tid >> 6);
  const int g = lane >> 4, l15 = lane & 15;
  const int xcd = bid & 7, idx = bid >> 3;
  const int rb = 8 * (xcd >> 2) + (idx >> 2);
  const int cb = (xcd & 3) + 4 * (idx & 3);
  const int rb2 = 4 * xcd + (idx >> 3);
  const int nu = idx & 7;
  const __amdgpu_buffer_rsrc_t wsr = __builtin_amdgcn_make_buffer_rsrc(ws, 0, (int)WS_END, 0x00020000);
  const unsigned lv = lane * 16;
  const unsigned ap = WS_AFRAG + (4 * rb2) * NKS * 1024;
  const unsigned lds0 = (unsigned)(size_t)smem;
  STAMP(0, 0);
  f32x4 acc[4][5];
  u64 tagf = 0;

  if (w < 4) {
    if (tid < 16) ((LAS unsigned*)(smem + CTRL))[tid] = 0u;
    u32x2 bb[7];
#pragma unroll
    for (int t = 0; t < 7; ++t) {
      int T = 14 * (cb - 9) + 7 * (w & 1) + t;
      T = T < 0 ? 0 : (T > 95 ? 95 : T);
      bb[t] = *(const u32x2*)(b1 + 16 * T + 4 * g);
    }
    const int aw = w;
    const u64 tag = load_tag(ws);
    if (cb < 8) p1_compute<true>(bb, ws, wsr, smem, rb, cb, w, tag);
    else p1_compute<false>(bb, ws, wsr, smem, rb, cb, w, tag);
    STAMP(0, 4);
    __builtin_amdgcn_s_setprio(1);
    STAMP(0, 5);
    lds_wait(smem, 1, 1);
#define F4(x) __builtin_bit_cast(f32x4, x)
    STAMP(0, 6);
    {
      const int pr = 5 * aw + (lane < 5 ? lane : 0);
      wait_flags(ws, WS_F1 + ((rb2 >> 1) * 16 + (pr < 16 ? pr >> 1 : 8)) * 8, true, tag);
    }
    bf16x8 P[5][4];
    u32x4 Z[5];
    {
      u32x4 zq[16];
      const unsigned zo = (unsigned)((40 * aw + (lane < 40 ? lane : 39)) * 16);
#pragma unroll
      for (int r = 0; r < 16; ++r) zq[r] = LD16(zo, WS_ZP + r * 2560);
      const unsigned a0 = ap + (48 + 5 * aw) * 1024;
#pragma unroll
      for (int tt = 0; tt < 5; ++tt)
#pragma unroll
        for (int mm = 0; mm < 4; ++mm) P[tt][mm] = __builtin_bit_cast(bf16x8, LD16(lv, a0 + (mm * NKS + tt) * 1024));
      SCHED();
      for (unsigned spins = 0;; ++spins) {
        unsigned mx = 0u;
#pragma unroll
        for (int r = 0; r < 16; ++r) mx = max(max(mx, max(zq[r][0], zq[r][1])), max(zq[r][2], zq[r][3]));
        if (__builtin_expect(__all(mx != 0xffffffffu) || spins > SPIN_CAP, 1)) break;
        __builtin_amdgcn_s_sleep(POLL_SLEEP);
#pragma unroll
        for (int r = 0; r < 16; ++r) zq[r] = LD16(zo, WS_ZP + r * 2560);
      }
      const f32x4 zs = (((F4(zq[0]) + F4(zq[1])) + (F4(zq[2]) + F4(zq[3]))) + ((F4(zq[4]) + F4(zq[5])) + (F4(zq[6]) + F4(zq[7])))) +
                       (((F4(zq[8]) + F4(zq[9])) + (F4(zq[10]) + F4(zq[11]))) + ((F4(zq[12]) + F4(zq[13])) + (F4(zq[14]) + F4(zq[15]))));
      LAS char* zl = smem + ASCR + w * 3072 + 1024;
      if (lane < 40) *(LAS u32x2*)(zl + (((lane >> 3) * 4 + (lane & 3)) * 2 + ((lane >> 2) & 1)) * 8) = (u32x2){pack2(zs[0], zs[1]), pack2(zs[2], zs[3])};
      asm volatile("s_waitcnt lgkmcnt(0)" ::: "memory");
#pragma unroll
      for (int t = 0; t < 5; ++t) Z[t] = *(LAS u32x4*)(zl + (t * 4 + g) * 16);
    }
    u32x4 Mf[3][5];
    const unsigned mo = WS_MFRAG + ((5 * nu) * 20 + 5 * aw) * 1024;
    {
      f32x4 Dn[4][3];
#pragma unroll
      for (int sg = 0; sg < 3; ++sg) {
        const int hr = ((l15 >> 2) + 4 * (4 * sg + (l15 & 3))) / 5;
#pragma unroll
        for (int m = 0; m < 4; ++m) Dn[m][sg] = (f32x4){0.f, 0.f, 0.f, 0.f};
#pragma unroll
        for (int t = 0; t < 5; ++t) {
          const bool used = (sg == 0 && t <= 2) || (sg == 1 && t >= 1) || (sg == 2 && t >= 3);
          if (used) {
            const bool k0 = ((8 * t + g) / 5) == hr, k1 = ((8 * t + 4 + g) / 5) == hr;
            u32x4 v = {k0 ? Z[t][0] : 0u, k0 ? Z[t][1] : 0u, k1 ? Z[t][2] : 0u, k1 ? Z[t][3] : 0u};
            const bf16x8 zm = __builtin_bit_cast(bf16x8, v);
#pragma unroll
            for (int m = 0; m < 4; ++m) Dn[m][sg] = MFMA16(zm, P[t][m], Dn[m][sg]);
          }
        }
      }
      SCHED();
#pragma unroll
      for (int t = 0; t < 3; ++t)
#pragma unroll
        for (int c = 0; c < 5; ++c) Mf[t][c] = LD16(lv, mo + (c * 20 + t) * 1024);
      SCHED();
#pragma unroll
      for (int m = 0; m < 4; ++m) {
#pragma unroll
        for (int t = 0; t < 5; ++t) {
          u32x4 p = __builtin_bit_cast(u32x4, P[t][m]);
          u32x4 q;
          const float r0 = __builtin_amdgcn_rcpf(Dn[m][(2 * t) >> 2][(2 * t) & 3] + 1e-6f);
          const float r1 = __builtin_amdgcn_rcpf(Dn[m][(2 * t + 1) >> 2][(2 * t + 1) & 3] + 1e-6f);
          q[0] = pack2(bflo(p[0]) * r0, bfhi(p[0]) * r0);
          q[1] = pack2(bflo(p[1]) * r0, bfhi(p[1]) * r0);
          q[2] = pack2(bflo(p[2]) * r1, bfhi(p[2]) * r1);
          q[3] = pack2(bflo(p[3]) * r1, bfhi(p[3]) * r1);
          P[t][m] = __builtin_bit_cast(bf16x8, q);
          asm volatile("" : "+v"(P[t][m]));
        }
      }
    }
    SCHED();
    STAMP(0, 7);
    {
      STAMP(1, 1);
      for (unsigned spins = 0;; ++spins) {
        unsigned mx = 0u;
#pragma unroll
        for (int t = 0; t < 3; ++t)
#pragma unroll
          for (int c = 0; c < 5; ++c) mx = max(max(mx, max(Mf[t][c][0], Mf[t][c][1])), max(Mf[t][c][2], Mf[t][c][3]));
        if (__builtin_expect(__all(mx != 0xffffffffu) || spins > SPIN_CAP, 1)) break;
        const int pl = lane < 40 ? lane : 39, pt = pl >> 3, pg = (pl >> 1) & 3, pe = pl & 1;
        const unsigned po = mo + ((pe ? 4 : 0) * 20 + pt) * 1024 + (16 * pg + (pe ? 15 : 0)) * 16;
        for (unsigned sp2 = 0;; ++sp2) {
          const unsigned v = __builtin_amdgcn_raw_buffer_load_b32(wsr, po, 0, 16);
          if (__all(v != 0xffffffffu) || sp2 > SPIN_CAP) break;
          __builtin_amdgcn_s_sleep(POLL_SLEEP);
        }
#pragma unroll
        for (int t = 0; t < 3; ++t)
#pragma unroll
          for (int c = 0; c < 5; ++c) Mf[t][c] = LD16(lv, mo + (c * 20 + t) * 1024);
      }
#pragma unroll
      for (int t = 0; t < 3; ++t) {
#pragma unroll
        for (int mm = 0; mm < 4; ++mm)
#pragma unroll
          for (int c = 0; c < 5; ++c)
            acc[mm][c] = MFMA16(__builtin_bit_cast(bf16x8, Mf[t][c]), P[t][mm], t == 0 ? ((f32x4){0.f, 0.f, 0.f, 0.f}) : acc[mm][c]);
        SCHED();
        if (t < 2) {
#pragma unroll
          for (int c = 0; c < 5; ++c) Mf[t][c] = LD16(lv, mo + (c * 20 + t + 3) * 1024);
        }
        SCHED();
      }
      for (unsigned spins = 0;; ++spins) {
        unsigned mx = 0u;
#pragma unroll
        for (int t = 0; t < 2; ++t)
#pragma unroll
          for (int c = 0; c < 5; ++c) mx = max(max(mx, max(Mf[t][c][0], Mf[t][c][1])), max(Mf[t][c][2], Mf[t][c][3]));
        if (__builtin_expect(__all(mx != 0xffffffffu) || spins > SPIN_CAP, 1)) break;
        __builtin_amdgcn_s_sleep(POLL_SLEEP);
#pragma unroll
        for (int t = 0; t < 2; ++t)
#pragma unroll
          for (int c = 0; c < 5; ++c) Mf[t][c] = LD16(lv, mo + (c * 20 + t + 3) * 1024);
      }
#pragma unroll
      for (int t = 3; t < 5; ++t)
#pragma unroll
        for (int mm = 0; mm < 4; ++mm)
#pragma unroll
          for (int c = 0; c < 5; ++c) acc[mm][c] = MFMA16(__builtin_bit_cast(bf16x8, Mf[t - 3][c]), P[t][mm], acc[mm][c]);
    }
    STAMP(1, 2);
    lds_wait(smem, 6, 4);
    {
      LAS char* p = smem + w * 20480 + lane * 16;
#pragma unroll
      for (int mm = 0; mm < 4; ++mm)
#pragma unroll
        for (int c = 0; c < 5; ++c) *(LAS f32x4*)(p + (mm * 5 + c) * 1024) = acc[mm][c];
    }
    BAR();
    return;
  } else {
    __builtin_amdgcn_s_setprio(3);
    const int lw = w - 4;
    u64 tag;
    {
      const unsigned xoff = (unsigned)((lane >> 3) * (D_ * 2) + (((lane & 7) ^ (lane >> 3)) << 4));
      const char* xg = (const char*)x + (size_t)(rb * 128 + 8 * lw) * (D_ * 2);
      const int ldw = cb <= 8 ? 1920 : F_;
      const int rl = lane >> 5, c = lane & 31;
      bool wact;
      int col;
      if (cb < 8) {
        const int cs = c - 2 * (cb & 1);
        col = c >= 24 ? 64 * cb + 8 * (c - 24) : (cs < 12 ? 640 + 80 * cb + 8 * cs : 1280 + 80 * cb + 8 * (cs - 12));
        wact = c >= 24 || (cs >= 0 && cs < 10) || (cs >= 12 && cs < 22);
      } else if (cb == 8) {
        const int wcc = c >= 14 ? 1 : 0, cc = c - 14 * wcc;
        col = 512 + 64 * wcc + 8 * cc;
        wact = c < 28 && cc < 8;
      } else {
        col = 224 * (cb - 9) + 8 * c;
        wact = c < 28 && col < F_;
      }
      if (!wact) col = 0;
      const unsigned woff = (unsigned)((rl * 16 * ldw + col) * 2);
      const char* wg = (const char*)(cb <= 8 ? wqkv : w1) + (size_t)lw * ldw * 2;
#define ROT1 1
      const int rot = ROT1 ? ((idx >> 2) + ((5 * (idx & 3)) >> 1) + 3 * xcd) % 10 : 0;
      auto stage = [&](int it, int buf) {
        int kt = it + rot; if (kt >= 10) kt -= 10;
        const unsigned sb = lds0 + buf * K1_STAGE;
        const char* xs = xg + kt * 128;
#pragma unroll
        for (int j = 0; j < 4; ++j) dma16(sb + (lw + 4 * j) * 1024, xoff, xs + j * (32 * D_ * 2));
        const char* wsrc = wg + (size_t)kt * 64 * ldw * 2;
        if (wact) {
#pragma unroll
          for (int j = 0; j < 8; ++j) {
            const int kj = 32 * (j >> 2) + 8 * (j & 1) + 4 * ((j >> 1) & 1);
            dma16(sb + K1_XB + (lw + 4 * j) * K1_WBLK, woff, wsrc + (size_t)kj * ldw * 2);
          }
        }
      };
      const int u2 = 4 * bid + lw, ks2 = u2 / 20, np2 = u2 - 20 * ks2;
      u32x4 w2a, w2b;
      {
        const u16* src = w2 + (size_t)(32 * (u2 < 960 ? ks2 : 0) + (lane >> 2)) * D_ + 32 * np2 + 8 * (lane & 3);
        asm volatile("global_load_dwordx4 %0, %2, off\n\tglobal_load_dwordx4 %1, %3, off"
                     : "=&v"(w2a), "=&v"(w2b) : "v"(src), "v"(src + 16 * D_) : "memory");
      }
      stage(0, 0);
      stage(1, 1);
      STAMP(0, 1);
      tag = load_tag(ws);
      asm volatile("s_waitcnt vmcnt(24)" : "+v"(w2a), "+v"(w2b) :: "memory");
      if (u2 < 960) {
        LAS char* t = smem + ASCR + lw * 3072;
        constexpr int RS = 96;
        *(LAS u32x4*)(t + (lane >> 2) * RS + (lane & 3) * 16) = w2a;
        *(LAS u32x4*)(t + (16 + (lane >> 2)) * RS + (lane & 3) * 16) = w2b;
        asm volatile("s_waitcnt lgkmcnt(0)" ::: "memory");
#pragma unroll
        for (int q = 0; q < 2; ++q) {
          const bf16x8 v = tr_frag(t + (4 * g + (l15 >> 2)) * RS + (l15 & 3) * 8, q * 32, 16 * RS);
          ST16(WS_W2F + ((2 * np2 + q) * 48 + ks2) * 1024 + lv, v);
        }
      }
    {
      const u32x4 sent = {0xffffffffu, 0xffffffffu, 0xffffffffu, 0xffffffffu};
      const int J = 32 * lw + idx, ks = xcd + 8 * (J / 40), gg = (J % 40) / 10, ct = J % 10, nl = 2 * (lane & 31) + (lane >> 5);
      if (J < 120 && ks < 20) ST16(WS_MFRAG + (((64 * ct + nl) >> 4) * 20 + ks) * 1024 + (16 * gg + (nl & 15)) * 16, sent);
      if (cb < 8 && lw < 2) {
        const unsigned kp = WS_KVP + rb * 51200 + (10 * (2 * cb + lw)) * 320;
#pragma unroll
        for (int q = 0; q < 3; ++q) ST16(kp + q * 1024 + lv, sent);
        if (lane < 8) ST16(kp + 3072 + lv, sent);
        if (lane < 10) ST16(WS_ZP + rb * 2560 + (10 * (2 * cb + lw)) * 16 + lv, sent);
      }
    }
      int nb = 2;
      u64 fe[4] = {0, 0, 0, 0};
#pragma unroll 1
      for (int i = 0; i < 8; ++i) {
        asm volatile("s_waitcnt vmcnt(12)" ::: "memory");
        __builtin_amdgcn_s_barrier();
        asm volatile("" ::: "memory");
        stage(i + 2, nb);
        nb = nb + 1; if (nb == 3) nb = 0;
        if (i == 2) arrive_publish(smem, 5, ws, WS_F0 + bid * 8, tag, 4, lane);
        if (DO_TOUCH && i == TOUCH_AT - 3 && lw == 0) {
          const char* fp = ws + WS_F0 + lane * 8;
          asm volatile("global_load_dwordx2 %0, %4, off sc1\n\tglobal_load_dwordx2 %1, %4, off offset:512 sc1\n\t"
                       "global_load_dwordx2 %2, %4, off offset:1024 sc1\n\tglobal_load_dwordx2 %3, %4, off offset:1536 sc1"
                       : "=&v"(fe[0]), "=&v"(fe[1]), "=&v"(fe[2]), "=&v"(fe[3]) : "v"(fp) : "memory");
        }
        if (DO_TOUCH && i == TOUCH_AT - 1 && lw == 0) {
          asm volatile("" : "+v"(fe[0]), "+v"(fe[1]), "+v"(fe[2]), "+v"(fe[3]));
          if (__all(fe[0] == tag && fe[1] == tag && fe[2] == tag && fe[3] == tag)) lds_signal(smem, 9, lane);
        }
      }
    }
    const int grow = rb * 128 + 8 * cb + 2 * lw;
    u32x4 gx0, gx1, gx2, gw0, gw1;
    {
      const u16* xr = x + (size_t)grow * D_;
      asm volatile("global_load_dwordx4 %0, %5, off\n\tglobal_load_dwordx4 %1, %5, off offset:1280\n\t"
                   "global_load_dwordx4 %2, %6, off\n\tglobal_load_dwordx4 %3, %7, off\n\tglobal_load_dwordx4 %4, %8, off"
                   : "=&v"(gx0), "=&v"(gx1), "=&v"(gx2), "=&v"(gw0), "=&v"(gw1)
                   : "v"(xr + 8 * lane), "v"(xr + ((lane >> 4) & 1) * D_ + 512 + 8 * (lane & 15)),
                     "v"(gate_w + 8 * lane), "v"(gate_w + 512 + 8 * (lane & 15)) : "memory");
    }
    asm volatile("s_waitcnt vmcnt(17)" ::: "memory");
    __builtin_amdgcn_s_barrier();
    asm volatile("" ::: "memory");
#define ROT2 1
    const int rot2 = ROT2 ? (3 * nu) >> 1 : 0;
    auto kso = [&](int s) { int k = s + rot2; return (unsigned)((k >= 12 ? k - 12 : k) * 1024); };
    const unsigned bo = WS_W2F + ((5 * nu) * 48 + 12 * lw) * 1024;
    auto slot_off = [&](int sl) { return sl < 3 ? RING1 + (3 * lw + sl) * 5120 : (3 * lw + sl - 3) * 5120; };
    auto fill = [&](int s, int sl) {
      const unsigned d = lds0 + slot_off(sl);
#pragma unroll
      for (int c = 0; c < 5; ++c) dma16_sc1(d + c * 1024, lv, ws + bo + c * 48 * 1024 + kso(s));
    };
    for (unsigned spins = 0;; ++spins) {
      u64 f0[4];
#pragma unroll
      for (int q = 0; q < 4; ++q) f0[q] = __hip_atomic_load((GAS u64*)(ws + WS_F0) + 64 * q + lane, __ATOMIC_RELAXED, __HIP_MEMORY_SCOPE_AGENT);
      if (__all(f0[0] == tag && f0[1] == tag && f0[2] == tag && f0[3] == tag) || spins > SPIN_CAP) break;
      __builtin_amdgcn_s_sleep(POLL_SLEEP);
    }
    asm volatile("s_waitcnt vmcnt(0)" ::: "memory");
    lds_signal(smem, 1, lane);
    __builtin_amdgcn_s_barrier();
    asm volatile("" ::: "memory");
    STAMP(0, 2);
    __builtin_amdgcn_s_setprio(2);
#define LDH(dst, soff) asm volatile("buffer_load_dwordx4 %0, %1, %2, %3 offen sc1" : "=v"(dst) : "v"(lv), "s"(wsr), "s"((unsigned)(soff)) : "memory")
    u32x4 Br[3][5];
#pragma unroll
    for (int s = 0; s < 3; ++s) fill(s, s);
    asm volatile("" : "+v"(gx0), "+v"(gx1), "+v"(gx2), "+v"(gw0), "+v"(gw1));
    {
      float s0 = 0.f, s1 = 0.f, s2 = 0.f;
#pragma unroll
      for (int j = 0; j < 4; ++j) {
        s0 += rbf(bflo(gx0[j]) * bflo(gw0[j])) + rbf(bfhi(gx0[j]) * bfhi(gw0[j]));
        s1 += rbf(bflo(gx1[j]) * bflo(gw0[j])) + rbf(bfhi(gx1[j]) * bfhi(gw0[j]));
        s2 += rbf(bflo(gx2[j]) * bflo(gw1[j])) + rbf(bfhi(gx2[j]) * bfhi(gw1[j]));
      }
      if (lane < 16) s0 += s2;
      else if (lane < 32) s1 += s2;
#pragma unroll
      for (int o = 32; o >= 1; o >>= 1) {
        s0 += __shfl_xor(s0, o);
        s1 += __shfl_xor(s1, o);
      }
      if (lane < 2) {
        float l = rbf(lane == 0 ? s0 : s1);
        float gv = rbf(1.f / (1.f + __expf(-l)));
        __builtin_amdgcn_raw_buffer_store_b32(__float_as_uint(gv), wsr, WS_G + (grow + lane) * 4, 0, 16);
      }
    }
    SCHED();
#pragma unroll
    for (int s = 0; s < 3; ++s)
#pragma unroll
      for (int c = 0; c < 5; ++c) LDH(Br[s][c], bo + c * 48 * 1024 + kso(6 + s));
    waitv<15>();
    arrive_publish(smem, 8, ws, WS_FG + (rb * 16 + cb) * 8, tag, 4, lane);
    lds_wait(smem, 2, 4);
#pragma unroll
    for (int s = 3; s < 6; ++s) fill(s, s);
    STAMP(0, 3);
    DRAIN();
#pragma unroll
    for (int s = 0; s < 3; ++s)
      asm volatile("" : "+v"(Br[s][0]), "+v"(Br[s][1]), "+v"(Br[s][2]), "+v"(Br[s][3]), "+v"(Br[s][4]));
    STAMP(0, 4);
    {
    const int J = 32 * lw + idx;
    const int ks = xcd + 8 * (J / 40);
    const bool job = J < 120 && ks < 20;
    const int gg = (J % 40) / 10, ct = J % 10;
    const int hf = lane >> 5, l31 = lane & 31;
    const unsigned mst = WS_MFRAG + (((64 * ct + 2 * l31 + hf) >> 4) * 20 + ks) * 1024 + (16 * gg + ((2 * l31 + hf) & 15)) * 16;
    const int m = 8 * ks + 4 * hf + gg, ph = m / 5;
    unsigned wv[20];
    if (job) {
#pragma unroll
      for (int e = 0; e < 20; ++e) wv[e] = *(const unsigned*)(wo + (size_t)(20 * ph + e) * D_ + 64 * ct + 2 * l31);
    }
    if (job) {
      LAS float* skv = (LAS float*)(smem + FSCR + lw * 640);
      for (unsigned spins = 0;; ++spins) {
        const unsigned v = __builtin_amdgcn_raw_buffer_load_b32(wsr, WS_KVP + (l31 & 15) * 51200 + m * 320, 0, 16);
        if (__all(v != 0xffffffffu) || spins > SPIN_CAP) break;
        __builtin_amdgcn_s_sleep(POLL_SLEEP);
      }
      const unsigned kvo = (unsigned)(m * 320 + (l31 < 20 ? l31 : 19) * 16);
      u32x4 t[16];
      for (unsigned spins = 0;; ++spins) {
#pragma unroll
        for (int r = 0; r < 16; ++r) t[r] = LD16(kvo, WS_KVP + r * 51200);
        unsigned mx = 0u;
#pragma unroll
        for (int r = 0; r < 16; ++r) mx = max(max(mx, max(t[r][0], t[r][1])), max(t[r][2], t[r][3]));
        if (__builtin_expect(__all(mx != 0xffffffffu) || spins > SPIN_CAP, 1)) break;
        __builtin_amdgcn_s_sleep(POLL_SLEEP);
      }
      const f32x4 s = (((F4(t[0]) + F4(t[1])) + (F4(t[2]) + F4(t[3]))) + ((F4(t[4]) + F4(t[5])) + (F4(t[6]) + F4(t[7])))) +
                      (((F4(t[8]) + F4(t[9])) + (F4(t[10]) + F4(t[11]))) + ((F4(t[12]) + F4(t[13])) + (F4(t[14]) + F4(t[15]))));
      if (l31 < 20) {
        const unsigned q0 = pack2(s[0], s[1]), q1 = pack2(s[2], s[3]);
        *(LAS f32x4*)(skv + hf * 80 + l31 * 4) = (f32x4){bflo(q0), bfhi(q0), bflo(q1), bfhi(q1)};
      }
      asm volatile("s_waitcnt lgkmcnt(0)" ::: "memory");
      f32x4 o0 = {0.f, 0.f, 0.f, 0.f}, o1 = {0.f, 0.f, 0.f, 0.f};
#pragma unroll
      for (int e = 0; e < 20; ++e) {
        f32x4 kvv = *(LAS f32x4*)(skv + hf * 80 + e * 4);
        o0 += kvv * bflo(wv[e]);
        o1 += kvv * bfhi(wv[e]);
      }
      const auto rx = __builtin_amdgcn_permlane32_swap(pack2(o0[0], o0[1]), pack2(o1[0], o1[1]), false, false);
      const auto ry = __builtin_amdgcn_permlane32_swap(pack2(o0[2], o0[3]), pack2(o1[2], o1[3]), false, false);
      const u32x4 v = {rx[0], ry[0], rx[1], ry[1]};
      ST16(mst, v);
    }
    }
    STAMP(0, 7);
    __builtin_amdgcn_s_setprio(1);
    wait_flags(ws, WS_F1 + ((rb2 >> 1) * 16 + 9 + (24 * lw + (lane < 24 ? lane : 0)) / 14) * 8, true, tag);
    STAMP(0, 5);
    u32x4 A[WIN][4];
    const unsigned a0 = ap + 12 * lw * 1024;
#pragma unroll
    for (int s = 0; s < WIN; ++s)
#pragma unroll
      for (int m = 0; m < 4; ++m) LDH(A[s][m], a0 + m * NKS * 1024 + kso(s));
#define FSTEP(s) do { \
      waitv<nwait(s)>(); \
      asm volatile("" : "+v"(A[(s) % WIN][0]), "+v"(A[(s) % WIN][1]), "+v"(A[(s) % WIN][2]), "+v"(A[(s) % WIN][3])); \
      bf16x8 B[5]; \
      if ((s) >= 6 && (s) < 9) { _Pragma("unroll") for (int c = 0; c < 5; ++c) B[c] = __builtin_bit_cast(bf16x8, Br[(s) >= 6 && (s) < 9 ? (s) - 6 : 0][c]); } \
      else { LAS char* sl = smem + slot_off((s) < 6 ? (s) : (s) - 9) + lv; \
             _Pragma("unroll") for (int c = 0; c < 5; ++c) B[c] = *(LAS bf16x8*)(sl + c * 1024); } \
      _Pragma("unroll") for (int c = 0; c < 5; ++c) \
        _Pragma("unroll") for (int m = 0; m < 4; ++m) \
          acc[m][c] = MFMA16(B[c], __builtin_bit_cast(bf16x8, A[(s) % WIN][m]), (s) == 0 ? ((f32x4){0.f, 0.f, 0.f, 0.f}) : acc[m][c]); \
      SCHED(); \
      if ((s) < 3) { asm volatile("s_waitcnt lgkmcnt(0)" ::: "memory"); fill((s) + 9, (s)); } \
      if ((s) == 5) lds_signal(smem, 6, lane); \
      if ((s) + WIN < 12) { _Pragma("unroll") for (int m = 0; m < 4; ++m) LDH(A[(s) % WIN][m], a0 + m * NKS * 1024 + kso((s) + WIN)); } \
      SCHED(); \
    } while (0)
    FSTEP(0); FSTEP(1); FSTEP(2); FSTEP(3); FSTEP(4); FSTEP(5);
    FSTEP(6); FSTEP(7); FSTEP(8); FSTEP(9); FSTEP(10); FSTEP(11);
    STAMP(0, 6);
    tagf = tag;
    if (bid == 0 && lw == 0) {
#pragma unroll
      for (int i = 0; i < 4; ++i) {
        wait_flags(ws, WS_F1 + (64 * i + lane) * 8, true, tag);
      }
      if (lane == 0) __hip_atomic_store((GAS u64*)(ws + WS_EPOCH), tag, __ATOMIC_RELAXED, __HIP_MEMORY_SCOPE_AGENT);
    }
  }
  {
    const int f = w - 4;
    asm volatile("s_waitcnt lgkmcnt(0)" ::: "memory");
#pragma unroll
    for (int m = 0; m < 4; ++m) {
      if (m != f) {
        LAS char* p = smem + RING1 + (3 * f + (m < f ? m : m - 1)) * 5120 + lv;
#pragma unroll
        for (int c = 0; c < 5; ++c) *(LAS f32x4*)(p + c * 1024) = acc[m][c];
      }
    }
    f32x4 fb[5];
#pragma unroll
    for (int m = 0; m < 4; ++m)
      if (m == f) {
#pragma unroll
        for (int c = 0; c < 5; ++c) fb[c] = acc[m][c];
      }
    const int row = 64 * rb2 + 16 * f + l15;
    wait_flags(ws, WS_FG + ((rb2 >> 1) * 16 + 8 * (rb2 & 1) + (lane & 7)) * 8, true, tagf);
    const float gv = __uint_as_float(__builtin_amdgcn_raw_buffer_load_b32(wsr, WS_G + row * 4, 0, 16));
    u32x2 bb[5];
#pragma unroll
    for (int c = 0; c < 5; ++c) bb[c] = *(const u32x2*)(b2 + 80 * nu + 16 * c + 4 * g);
    BAR();
    STAMP(1, 4);
    const float om = rbf(1.f - gv);
#pragma unroll
    for (int c = 0; c < 5; ++c) {
      LAS char* pa = smem + (f * 5 + c) * 1024 + lv;
      const f32x4 fa = (*(LAS f32x4*)(pa) + *(LAS f32x4*)(pa + 20480)) + (*(LAS f32x4*)(pa + 2 * 20480) + *(LAS f32x4*)(pa + 3 * 20480));
#pragma unroll
      for (int o = 1; o < 4; ++o) {
        const int src = (f + o) & 3;
        fb[c] += *(LAS f32x4*)(smem + RING1 + (3 * src + (f < src ? f : f - 1)) * 5120 + c * 1024 + lv);
      }
      const int n = 80 * nu + 16 * c + 4 * g;
      const unsigned b01 = pack2(fb[c][0], fb[c][1]), b23 = pack2(fb[c][2], fb[c][3]);
      const unsigned y01 = pack2(bflo(b01) + bflo(bb[c][0]), bfhi(b01) + bfhi(bb[c][0]));
      const unsigned y23 = pack2(bflo(b23) + bflo(bb[c][1]), bfhi(b23) + bfhi(bb[c][1]));
      const unsigned a01 = pack2(fa[0], fa[1]), a23 = pack2(fa[2], fa[3]);
      const unsigned ga01 = pack2(gv * bflo(a01), gv * bfhi(a01)), ga23 = pack2(gv * bflo(a23), gv * bfhi(a23));
      const unsigned gb01 = pack2(om * bflo(y01), om * bfhi(y01)), gb23 = pack2(om * bflo(y23), om * bfhi(y23));
      u32x2 v = {pack2(bflo(ga01) + bflo(gb01), bfhi(ga01) + bfhi(gb01)), pack2(bflo(ga23) + bflo(gb23), bfhi(ga23) + bfhi(gb23))};
      *(u32x2*)(out + (size_t)row * D_ + n) = v;
    }
  }
  STAMP(1, 5);
}

extern "C" void kernel_launch(void* const* d_in, const int* in_sizes, int n_in, void* d_out, int out_size,
                              void* d_ws, size_t ws_size, hipStream_t stream) {
  const u16* x = (const u16*)d_in[0];
  const u16* wqkv = (const u16*)d_in[1];
  const u16* wo = (const u16*)d_in[2];
  const u16* w1 = (const u16*)d_in[4];
  const u16* b1 = (const u16*)d_in[5];
  const u16* w2 = (const u16*)d_in[6];
  const u16* b2 = (const u16*)d_in[7];
  const u16* gw = (const u16*)d_in[8];
  (void)hipFuncSetAttribute((const void*)fused_kernel, hipFuncAttributeMaxDynamicSharedMemorySize, LDS_BYTES);
  fused_kernel<<<256, 512, LDS_BYTES, stream>>>(x, wqkv, wo, w1, b1, w2, b2, gw, (char*)d_ws, (u16*)d_out);
}
```

```cpp
#include <hip/hip_runtime.h>
#include <hip/hip_bf16.h>
#include <stdint.h>

typedef __attribute__((ext_vector_type(8))) short bf16x8;
typedef __attribute__((ext_vector_type(4))) short s16x4;
typedef __attribute__((ext_vector_type(4))) float f32x4;
typedef __attribute__((ext_vector_type(2))) float f32x2;
typedef __attribute__((ext_vector_type(2))) __bf16 bf16x2v;
typedef __attribute__((ext_vector_type(4))) unsigned u32x4;
typedef __attribute__((ext_vector_type(2))) unsigned u32x2;
typedef unsigned short u16;
#define LAS __attribute__((address_space(3)))
#define GAS __attribute__((address_space(1)))
#define DI __device__ __forceinline__

constexpr int S_ = 2048, D_ = 640, F_ = 1536;
constexpr int NKS = 68;

constexpr size_t WS_CNT = 0;
constexpr size_t WS_AFRAG = 4096;
constexpr size_t WS_BFRAG = WS_AFRAG + (size_t)128 * NKS * 1024;
constexpr size_t WS_KVP = WS_BFRAG + (size_t)40 * NKS * 1024;
constexpr size_t WS_ZP = WS_KVP + (size_t)16 * 51200;
constexpr size_t WS_G = WS_ZP + 16 * 2560;
constexpr size_t WS_END = WS_G + 2048 * 4;

DI unsigned pack2(float a, float b) {
  f32x2 f = {a, b};
  return __builtin_bit_cast(unsigned, __builtin_convertvector(f, bf16x2v));
}
DI float rbf(float a) { return __uint_as_float(pack2(a, a) << 16); }
DI float bflo(unsigned u) { return __uint_as_float(u << 16); }
DI float bfhi(unsigned u) { return __uint_as_float(u & 0xffff0000u); }
typedef __attribute__((ext_vector_type(2))) short s16x2;
DI unsigned phi2(float a0, float a1) {
  const unsigned p = pack2(a0, a1);
  const float t0 = bflo(p), t1 = bfhi(p);
  const float e0 = __builtin_amdgcn_exp2f(fminf(t0 * 1.44269504f, 0.f));
  const float e1 = __builtin_amdgcn_exp2f(fminf(t1 * 1.44269504f, 0.f));
  return pack2((fmaxf(t0, 0.f) + e0) + 1e-6f, (fmaxf(t1, 0.f) + e1) + 1e-6f);
}
DI unsigned sqrelu2(float a0, float a1) {
  unsigned q = pack2(a0, a1);
  const s16x2 z = {0, 0};
  q = __builtin_bit_cast(unsigned, __builtin_elementwise_max(__builtin_bit_cast(s16x2, q), z));
  const float u0 = bflo(q), u1 = bfhi(q);
  return pack2(u0 * u0, u1 * u1);
}
#define STAMP(kern,i)
#define ITS_DECL
#define ITS(i, k)
#define ITS_DUMP
#define MFMA16(a, b, c) __builtin_amdgcn_mfma_f32_16x16x32_bf16((a), (b), (c), 0, 0, 0)
#define SCHED() __builtin_amdgcn_sched_barrier(0)
#define SGB(mask, n) __builtin_amdgcn_sched_group_barrier(mask, n, 0)
#define INTERLEAVE() do { _Pragma("unroll") for (int q_ = 0; q_ < 18; ++q_) { SGB(0x100, 1); SGB(0x8, 1); } SGB(0x8, 10); } while (0)
#define STMODE 0
#define ST16(p, v) (*(u32x4*)(p) = (v))
#define ST8(p, v) (*(u32x2*)(p) = (v))

constexpr int K1_XB = 16384;
constexpr int K1_WBLK = 1056;
constexpr int K1_WGRP = 8 * K1_WBLK;
constexpr int K1_WB = 32 * K1_WBLK;
constexpr int K1_STAGE = K1_XB + K1_WB;
constexpr int K1_SCR = 3 * K1_STAGE;
constexpr int K1_LDS = K1_SCR + 4 * 3072;

DI void dma16(unsigned lds_off, unsigned voff, const char* gbase) {
  const unsigned lo = __builtin_amdgcn_readfirstlane((unsigned)(uintptr_t)gbase);
  const unsigned hi = __builtin_amdgcn_readfirstlane((unsigned)((uintptr_t)gbase >> 32));
  const unsigned long long gb = ((unsigned long long)hi << 32) | lo;
  const unsigned lo2 = __builtin_amdgcn_readfirstlane(lds_off);
  asm volatile("s_mov_b32 m0, %0\n\ts_nop 3\n\tglobal_load_lds_dwordx4 %1, %2"
               :: "s"(lo2), "v"(voff), "s"(gb) : "memory");
}

DI bf16x8 tr_frag(LAS char* p0, int off) {
  s16x4 lo = __builtin_amdgcn_ds_read_tr16_b64_v4i16((LAS s16x4*)(p0 + off));
  s16x4 hi = __builtin_amdgcn_ds_read_tr16_b64_v4i16((LAS s16x4*)(p0 + off + K1_WGRP));
  return __builtin_shufflevector(lo, hi, 0, 1, 2, 3, 4, 5, 6, 7);
}

template <bool KV>
DI void k1_compute(const u16* __restrict__ b1, char* __restrict__ ws, LAS char* smem, int rb, int cb, int w) {
  const int lane = threadIdx.x & 63;
  const int wr = w >> 1, wc = w & 1;
  const int g = lane >> 4, l15 = lane & 15;

  f32x4 acc[7][4];
#pragma unroll
  for (int t = 0; t < 7; ++t) {
    f32x4 init = {0.f, 0.f, 0.f, 0.f};
    if (!KV) {
      int T = 14 * (cb - 9) + 7 * wc + t;
      T = T < 0 ? 0 : (T > 95 ? 95 : T);
      const u32x2 bb = *(const u32x2*)(b1 + 16 * T + 4 * g);
      if (cb > 8) init = (f32x4){bflo(bb[0]), bfhi(bb[0]), bflo(bb[1]), bfhi(bb[1])};
    }
#pragma unroll
    for (int m = 0; m < 4; ++m) acc[t][m] = init;
  }
  const int xrd = (wr * 64 + l15) * 128;
  const int xsw = l15 & 7;
  const int wrow = K1_XB + (4 * (g & 1) + (l15 >> 2)) * K1_WBLK + (g >> 1) * 512;
  const int ka = 32 * (cb & 1);
  const int wrd = wrow + (KV ? ka + wc * 80 : wc * 224) + (l15 & 3) * 8;
  const int wrdq = wrow + 384 + wc * 64 + (l15 & 3) * 8;
  const int wrd4 = wrow + ka + wc * 80 + 32 + (l15 & 1) * 40 + ((l15 >> 1) & 1) * 192;

  bf16x8 a0[4], b0[7], a1[4], b1f[7];
  auto rd = [&](LAS char* sb, int s, bf16x8 (&a)[4], bf16x8 (&b)[7]) {
#pragma unroll
    for (int m = 0; m < 4; ++m) a[m] = *(LAS bf16x8*)(sb + xrd + m * 2048 + (((4 * s + g) ^ xsw) << 4));
    if (KV) {
      b[0] = tr_frag(sb + wrd, s * 2 * K1_WGRP + 0);
      b[1] = tr_frag(sb + wrd, s * 2 * K1_WGRP + 40);
      b[2] = tr_frag(sb + wrd, s * 2 * K1_WGRP + 192);
      b[3] = tr_frag(sb + wrd, s * 2 * K1_WGRP + 232);
      b[4] = tr_frag(sb + wrd4, s * 2 * K1_WGRP);
      b[5] = tr_frag(sb + wrdq, s * 2 * K1_WGRP);
      b[6] = tr_frag(sb + wrdq, s * 2 * K1_WGRP + 32);
    } else {
#pragma unroll
      for (int t = 0; t < 7; ++t) b[t] = tr_frag(sb + wrd, s * 2 * K1_WGRP + 32 * t);
    }
  };
  auto mm = [&](bf16x8 (&a)[4], bf16x8 (&b)[7]) {
#pragma unroll
    for (int t = 0; t < 7; ++t)
#pragma unroll
      for (int m = 0; m < 4; ++m) {
        if (KV && t < 5) acc[t][m] = MFMA16(a[m], b[t], acc[t][m]);
        else acc[t][m] = MFMA16(b[t], a[m], acc[t][m]);
      }
  };
  __builtin_amdgcn_s_barrier();
  asm volatile("" ::: "memory");
  rd(smem, 0, a0, b0);
  rd(smem, 1, a1, b1f);
  mm(a0, b0);
  int buf = 1;
#pragma unroll 1
  for (int i = 1; i < 10; ++i) {
    asm volatile("s_waitcnt lgkmcnt(0)" ::: "memory");
    __builtin_amdgcn_s_barrier();
    asm volatile("" ::: "memory");
    LAS char* sb = smem + buf * K1_STAGE;
    SCHED();
    rd(sb, 0, a0, b0);
    mm(a1, b1f);
    INTERLEAVE();
    SCHED();
    rd(sb, 1, a1, b1f);
    mm(a0, b0);
    INTERLEAVE();
    SCHED();
    buf = buf + 1; if (buf == 3) buf = 0;
  }
  mm(a1, b1f);
  STAMP(0, 3);

  char* afrag = ws + WS_AFRAG;
  const int rt0 = rb * 8 + wr * 4;
  if (KV) {
    const int hp = 2 * cb + wc;
    f32x4 R[10];
#pragma unroll
    for (int t = 0; t < 10; ++t) R[t] = (f32x4){0.f, 0.f, 0.f, 0.f};
    const bool iskl = l15 < 8;
    const bf16x8 ones = {0x3f80, 0x3f80, 0x3f80, 0x3f80, 0x3f80, 0x3f80, 0x3f80, 0x3f80};
#pragma unroll
    for (int u = 0; u < 2; ++u) {
      bf16x8 T[5];
#pragma unroll
      for (int t = 0; t < 5; ++t) {
        u32x4 p;
#pragma unroll
        for (int j = 0; j < 4; ++j) {
          const float e0 = acc[t][2 * u + (j >> 1)][2 * (j & 1)], e1 = acc[t][2 * u + (j >> 1)][2 * (j & 1) + 1];
          if (t < 2) p[j] = phi2(e0, e1);
          else if (t < 4) p[j] = pack2(e0, e1);
          else p[j] = iskl ? phi2(e0, e1) : pack2(e0, e1);
        }
        T[t] = __builtin_bit_cast(bf16x8, p);
      }
      R[0] = MFMA16(T[0], T[2], R[0]);
      R[1] = MFMA16(T[0], T[4], R[1]);
      R[2] = MFMA16(T[4], T[2], R[2]);
      R[3] = MFMA16(T[1], T[3], R[3]);
      R[4] = MFMA16(T[1], T[4], R[4]);
      R[5] = MFMA16(T[4], T[3], R[5]);
      R[6] = MFMA16(T[4], T[4], R[6]);
      R[7] = MFMA16(T[0], ones, R[7]);
      R[8] = MFMA16(T[1], ones, R[8]);
      R[9] = MFMA16(T[4], ones, R[9]);
    }
    LAS char* red = smem + K1_STAGE + wc * 10240;
    if (wr == 1) {
#pragma unroll
      for (int t = 0; t < 10; ++t) *(LAS f32x4*)(red + t * 1024 + lane * 16) = R[t];
    }
#pragma unroll
    for (int m = 0; m < 4; ++m) {
      u32x4 v;
      v[0] = phi2(acc[5][m][0], acc[5][m][1]);
      v[1] = phi2(acc[5][m][2], acc[5][m][3]);
      v[2] = phi2(acc[6][m][0], acc[6][m][1]);
      v[3] = phi2(acc[6][m][2], acc[6][m][3]);
      ST16(afrag + ((size_t)(rt0 + m) * NKS + 48 + hp) * 1024 + lane * 16, v);
    }
    asm volatile("s_waitcnt lgkmcnt(0)" ::: "memory");
    __builtin_amdgcn_s_barrier();
    asm volatile("" ::: "memory");
    if (wr == 0) {
#pragma unroll
      for (int t = 0; t < 10; ++t) R[t] += *(LAS f32x4*)(red + t * 1024 + lane * 16);
      char* kp = ws + WS_KVP + (size_t)rb * 51200 + (size_t)(10 * hp) * 320;
      *(f32x4*)(kp + g * 320 + l15 * 16) = R[0];
      *(f32x4*)(kp + 1600 + g * 320 + l15 * 16) = R[3];
      if (l15 >= 8) *(f32x4*)(kp + (l15 >= 12 ? 1600 : 0) + g * 320 + (16 + (l15 & 3)) * 16) = l15 >= 12 ? R[4] : R[1];
      if (g < 2) *(f32x4*)(kp + g * 1600 + 4 * 320 + l15 * 16) = g ? R[5] : R[2];
      if (g < 2 && (l15 >> 2) == 2 + g) *(f32x4*)(kp + g * 1600 + 4 * 320 + (16 + (l15 & 3)) * 16) = R[6];
      char* zp = ws + WS_ZP + (size_t)rb * 2560 + (size_t)(10 * hp) * 16;
      if (l15 < 2 || (l15 == 2 && g < 2))
        *(f32x4*)(zp + (l15 == 0 ? g * 16 : (l15 == 1 ? 80 + g * 16 : g * 80 + 64))) = l15 == 0 ? R[7] : (l15 == 1 ? R[8] : R[9]);
    }
  } else {
    if (cb == 8) {
#pragma unroll
      for (int pp = 0; pp < 2; ++pp)
#pragma unroll
        for (int m = 0; m < 4; ++m) {
          u32x4 v;
          v[0] = phi2(acc[2 * pp][m][0], acc[2 * pp][m][1]);
          v[1] = phi2(acc[2 * pp][m][2], acc[2 * pp][m][3]);
          v[2] = phi2(acc[2 * pp + 1][m][0], acc[2 * pp + 1][m][1]);
          v[3] = phi2(acc[2 * pp + 1][m][2], acc[2 * pp + 1][m][3]);
          ST16(afrag + ((size_t)(rt0 + m) * NKS + 48 + 16 + 2 * wc + pp) * 1024 + lane * 16, v);
        }
    } else {
      const int T0 = 14 * (cb - 9) + 7 * wc;
      u32x2 hv[7][4];
#pragma unroll
      for (int t = 0; t < 7; ++t) {
#pragma unroll
        for (int m = 0; m < 4; ++m) {
          hv[t][m][0] = sqrelu2(acc[t][m][0], acc[t][m][1]);
          hv[t][m][1] = sqrelu2(acc[t][m][2], acc[t][m][3]);
        }
      }
      char* base = afrag + ((size_t)rt0 * NKS + (T0 >> 1)) * 1024 + lane * 16;
      if (wc == 0) {
#pragma unroll
        for (int m = 0; m < 4; ++m) {
          char* bm = base + (size_t)m * NKS * 1024;
#pragma unroll
          for (int pp = 0; pp < 3; ++pp) {
            u32x4 v = {hv[2 * pp][m][0], hv[2 * pp][m][1], hv[2 * pp + 1][m][0], hv[2 * pp + 1][m][1]};
            ST16(bm + pp * 1024, v);
          }
          ST8(bm + 3 * 1024, hv[6][m]);
        }
      } else {
        const bool full = T0 + 6 < 96;
#pragma unroll
        for (int m = 0; m < 4; ++m) {
          char* bm = base + (size_t)m * NKS * 1024;
          ST8(bm + 8, hv[0][m]);
#pragma unroll
          for (int pp = 0; pp < 3; ++pp) {
            u32x4 v = {hv[2 * pp + 1][m][0], hv[2 * pp + 1][m][1], hv[2 * pp + 2][m][0], hv[2 * pp + 2][m][1]};
            if (pp < 2 || full) ST16(bm + (pp + 1) * 1024, v);
          }
        }
      }
    }
  }
}

__global__ void __launch_bounds__(512, 2)
k1_kernel(const u16* __restrict__ x, const u16* __restrict__ wqkv, const u16* __restrict__ w1,
          const u16* __restrict__ b1, const u16* __restrict__ w2, const u16* __restrict__ gate_w,
          char* __restrict__ ws) {
  extern __shared__ __attribute__((aligned(16))) char smem_[];
  LAS char* smem = (LAS char*)smem_;
  const int bid = blockIdx.x, tid = threadIdx.x, lane = tid & 63;
  const int w = __builtin_amdgcn_readfirstlane(tid >> 6);
  const int xcd = bid & 7, idx = bid >> 3;
  const int rb = 8 * (xcd >> 2) + (idx >> 2);
  const int cb = (xcd & 3) + 4 * (idx & 3);
  STAMP(0, 0);

  if (w < 4) {
    if (cb < 8) k1_compute<true>(b1, ws, smem, rb, cb, w);
    else k1_compute<false>(b1, ws, smem, rb, cb, w);
    STAMP(0, 4);
    return;
  }
  __builtin_amdgcn_s_setprio(3);
  const int lw = w - 4;
  const unsigned lds0 = (unsigned)(size_t)smem;
  const unsigned xoff = (unsigned)((lane >> 3) * (D_ * 2) + (((lane & 7) ^ (lane >> 3)) << 4));
  const char* xg = (const char*)x + (size_t)(rb * 128 + 8 * lw) * (D_ * 2);
  const int ldw = cb <= 8 ? 1920 : F_;
  const int rl = lane >> 5, c = lane & 31;
  bool wact;
  int col;
  if (cb < 8) {
    const int cs = c - 2 * (cb & 1);
    col = c >= 24 ? 64 * cb + 8 * (c - 24) : (cs < 12 ? 640 + 80 * cb + 8 * cs : 1280 + 80 * cb + 8 * (cs - 12));
    wact = c >= 24 || (cs >= 0 && cs < 10) || (cs >= 12 && cs < 22);
  } else if (cb == 8) {
    const int wcc = c >= 14 ? 1 : 0, cc = c - 14 * wcc;
    col = 512 + 64 * wcc + 8 * cc;
    wact = c < 28 && cc < 8;
  } else {
    col = 224 * (cb - 9) + 8 * c;
    wact = c < 28 && col < F_;
  }
  if (!wact) col = 0;
  const unsigned woff = (unsigned)((rl * 16 * ldw + col) * 2);
  const char* wg = (const char*)(cb <= 8 ? wqkv : w1) + (size_t)lw * ldw * 2;

  const int rot = ((idx >> 2) + ((5 * (idx & 3)) >> 1) + 3 * xcd) % 10;
  auto stage = [&](int it, int buf) {
    int kt = it + rot; if (kt >= 10) kt -= 10;
    const unsigned sb = lds0 + buf * K1_STAGE;
    const char* xs = xg + kt * 128;
#pragma unroll
    for (int j = 0; j < 4; ++j) dma16(sb + (lw + 4 * j) * 1024, xoff, xs + j * (32 * D_ * 2));
    const char* wsrc = wg + (size_t)kt * 64 * ldw * 2;
    if (wact) {
#pragma unroll
      for (int j = 0; j < 8; ++j) {
        const int kj = 32 * (j >> 2) + 8 * (j & 1) + 4 * ((j >> 1) & 1);
        dma16(sb + K1_XB + (lw + 4 * j) * K1_WBLK, woff, wsrc + (size_t)kj * ldw * 2);
      }
    }
  };
  stage(0, 0);
  stage(1, 1);
  STAMP(0, 1);
  ITS_DECL
  ITS(0, 0);
  asm volatile("s_waitcnt vmcnt(12)" ::: "memory");
  ITS(0, 1);
  __builtin_amdgcn_s_barrier();
  asm volatile("" ::: "memory");
  ITS(0, 2);
  stage(2, 2);
  ITS(0, 3);

  int nb = 0;
#pragma unroll 1
  for (int i = 1; i < 8; ++i) {
    ITS(i, 0);
    asm volatile("s_waitcnt vmcnt(12)" ::: "memory");
    ITS(i, 1);
    __builtin_amdgcn_s_barrier();
    asm volatile("" ::: "memory");
    ITS(i, 2);
    stage(i + 2, nb);
    ITS(i, 3);
    nb = nb + 1; if (nb == 3) nb = 0;
  }
  asm volatile("s_waitcnt vmcnt(12)" ::: "memory");
  __builtin_amdgcn_s_barrier();
  asm volatile("s_waitcnt vmcnt(0)" ::: "memory");
  __builtin_amdgcn_s_barrier();
  asm volatile("" ::: "memory");
  STAMP(0, 2);
  __builtin_amdgcn_s_setprio(0);
  const int u2 = 4 * bid + lw;
  const int ks2 = u2 / 20, np2 = u2 - 20 * ks2;
  u32x4 w2a = {0, 0, 0, 0}, w2b = {0, 0, 0, 0};
  if (u2 < 960) {
    const u16* src = w2 + (size_t)(32 * ks2 + (lane >> 2)) * D_ + 32 * np2 + 8 * (lane & 3);
    w2a = *(const u32x4*)src;
    w2b = *(const u32x4*)(src + 16 * D_);
  }
  const int grow = rb * 128 + 8 * cb + 2 * lw;
  u32x4 gx0 = *(const u32x4*)(x + (size_t)grow * D_ + 8 * lane);
  u32x4 gx1 = *(const u32x4*)(x + (size_t)(grow + 1) * D_ + 8 * lane);
  u32x4 gw0 = *(const u32x4*)(gate_w + 8 * lane);
  u32x4 gx2 = *(const u32x4*)(x + (size_t)(grow + ((lane >> 4) & 1)) * D_ + 512 + 8 * (lane & 15));
  u32x4 gw1 = *(const u32x4*)(gate_w + 512 + 8 * (lane & 15));
  {
    const u32x4 sent = {0xffffffffu, 0xffffffffu, 0xffffffffu, 0xffffffffu};
    if (u2 < 800) {
      const int nt = u2 / 20, ks = u2 - 20 * nt;
      *(u32x4*)(ws + WS_BFRAG + ((size_t)nt * NKS + 48 + ks) * 1024 + lane * 16) = sent;
    }
  }

  {
    float s0 = 0.f, s1 = 0.f, s2 = 0.f;
#pragma unroll
    for (int j = 0; j < 4; ++j) {
      s0 += rbf(bflo(gx0[j]) * bflo(gw0[j])) + rbf(bfhi(gx0[j]) * bfhi(gw0[j]));
      s1 += rbf(bflo(gx1[j]) * bflo(gw0[j])) + rbf(bfhi(gx1[j]) * bfhi(gw0[j]));
      s2 += rbf(bflo(gx2[j]) * bflo(gw1[j])) + rbf(bfhi(gx2[j]) * bfhi(gw1[j]));
    }
    if (lane < 16) s0 += s2;
    else if (lane < 32) s1 += s2;
#pragma unroll
    for (int o = 32; o >= 1; o >>= 1) {
      s0 += __shfl_xor(s0, o);
      s1 += __shfl_xor(s1, o);
    }
    if (lane < 2) {
      float l = rbf(lane == 0 ? s0 : s1);
      float gv = rbf(1.f / (1.f + __expf(-l)));
      ((float*)(ws + WS_G))[grow + lane] = gv;
    }
  }
  if (u2 < 960) {
    LAS char* t = smem + K1_SCR + lw * 3072;
    constexpr int RS = 96;
    *(LAS u32x4*)(t + (lane >> 2) * RS + (lane & 3) * 16) = w2a;
    *(LAS u32x4*)(t + (16 + (lane >> 2)) * RS + (lane & 3) * 16) = w2b;
    asm volatile("s_waitcnt lgkmcnt(0)" ::: "memory");
    const int g = lane >> 4, l15 = lane & 15;
#pragma unroll
    for (int i = 0; i < 2; ++i) {
      LAS char* p = t + (4 * g + (l15 >> 2)) * RS + i * 32 + (l15 & 3) * 8;
      s16x4 lo = __builtin_amdgcn_ds_read_tr16_b64_v4i16((LAS s16x4*)p);
      s16x4 hi = __builtin_amdgcn_ds_read_tr16_b64_v4i16((LAS s16x4*)(p + 16 * RS));
      bf16x8 v = __builtin_shufflevector(lo, hi, 0, 1, 2, 3, 4, 5, 6, 7);
      *(bf16x8*)(ws + WS_BFRAG + ((size_t)(2 * np2 + i) * NKS + ks2) * 1024 + lane * 16) = v;
    }
  }

  ITS_DUMP
  if (cb < 8) __builtin_amdgcn_s_barrier();
  STAMP(0, 4);
}

constexpr int K2_LDS = 8 * 20 * 1024;
#define WINF 3
constexpr int WIN = WINF;
#define APRIO 1
#define MAUX 0

__global__ void __launch_bounds__(512, 2)
k2_kernel(const u16* __restrict__ wo, const u16* __restrict__ b2, char* ws, u16* __restrict__ out) {
  extern __shared__ __attribute__((aligned(16))) char smem_[];
  LAS char* smem = (LAS char*)smem_;
  const int bid = blockIdx.x, tid = threadIdx.x, lane = tid & 63;
  const int w = __builtin_amdgcn_readfirstlane(tid >> 6);
  const int g = lane >> 4, l15 = lane & 15;
  const int xcd = bid & 7, idx = bid >> 3;
  const int rb = 8 * (xcd >> 1) + (idx >> 2);
  const int nu = 4 * (xcd & 1) + (idx & 3);
  const __amdgpu_buffer_rsrc_t wsr = __builtin_amdgcn_make_buffer_rsrc(ws, 0, (int)WS_END, 0x00020000);
  const unsigned lv = lane * 16;
  const unsigned ap = (unsigned)(WS_AFRAG + (size_t)(4 * rb) * NKS * 1024);
#define LDF(soff) __builtin_bit_cast(bf16x8, __builtin_amdgcn_raw_buffer_load_b128(wsr, lv, (soff), 0))
  STAMP(1, 0);
  if (w < 4) {
    f32x4 acc[4][5];
#pragma unroll
    for (int m = 0; m < 4; ++m)
#pragma unroll
      for (int c = 0; c < 5; ++c) acc[m][c] = (f32x4){0.f, 0.f, 0.f, 0.f};
    bf16x8 A[WIN][4], B[WIN][5];
    const unsigned a0 = ap + 12 * w * 1024;
    const unsigned b0 = (unsigned)(WS_BFRAG + (size_t)(5 * nu) * NKS * 1024) + 12 * w * 1024;
#pragma unroll
    for (int s = 0; s < WIN; ++s) {
      const int so = s;
#pragma unroll
      for (int m = 0; m < 4; ++m) A[s][m] = LDF(a0 + (m * NKS + so) * 1024);
#pragma unroll
      for (int c = 0; c < 5; ++c) B[s][c] = LDF(b0 + (c * NKS + so) * 1024);
    }
    SCHED();
#pragma unroll
    for (int s = 0; s < 12; ++s) {
#pragma unroll
      for (int m = 0; m < 4; ++m)
#pragma unroll
        for (int c = 0; c < 5; ++c) acc[m][c] = MFMA16(B[s % WIN][c], A[s % WIN][m], acc[m][c]);
      SCHED();
      if (s + WIN < 12) {
        const int so = s + WIN;
#pragma unroll
        for (int m = 0; m < 4; ++m) A[s % WIN][m] = LDF(a0 + (m * NKS + so) * 1024);
#pragma unroll
        for (int c = 0; c < 5; ++c) B[s % WIN][c] = LDF(b0 + (c * NKS + so) * 1024);
      }
      SCHED();
    }
    STAMP(1, 2);
    LAS char* p = smem + w * 20480 + lane * 16;
#pragma unroll
    for (int m = 0; m < 4; ++m)
#pragma unroll
      for (int c = 0; c < 5; ++c) *(LAS f32x4*)(p + (m * 5 + c) * 1024) = acc[m][c];
  } else {
    __builtin_amdgcn_s_setprio(3);
    const int aw = w - 4;
    const int J = 32 * aw + idx;
    const int ksj = xcd + 8 * (J / 40);
    const bool job = J < 120 && ksj < 20;
    bf16x8 P[5][4];
    const unsigned a0 = ap + (48 + 5 * aw) * 1024;
    f32x4 zq[16];
    const unsigned zo = (unsigned)((40 * aw + (lane < 40 ? lane : 39)) * 16);
#define LOADZP() _Pragma("unroll") for (int r = 0; r < 16; ++r) \
      zq[r] = __builtin_bit_cast(f32x4, __builtin_amdgcn_raw_buffer_load_b128(wsr, zo, (unsigned)(WS_ZP + (size_t)r * 2560), 0))
    if (job) {
      const int ks = ksj;
      const int rem = J % 40, gg = rem / 10, ct = rem - 10 * gg;
      const int hf = lane >> 5, l31 = lane & 31;
      const int m = 8 * ks + 4 * hf + gg, ph = m / 5, dq = m - 5 * ph;
      LAS float* skv = (LAS float*)(smem + w * 20480);
      const unsigned kvo = (unsigned)(m * 320 + (l31 < 20 ? l31 : 19) * 16);
      f32x4 t[16];
#pragma unroll
      for (int r = 0; r < 16; ++r)
        t[r] = __builtin_bit_cast(f32x4, __builtin_amdgcn_raw_buffer_load_b128(wsr, kvo, (unsigned)(WS_KVP + (size_t)r * 51200), 0));
      unsigned wv[20];
#pragma unroll
      for (int e = 0; e < 20; ++e) wv[e] = *(const unsigned*)(wo + (size_t)(20 * ph + e) * D_ + 64 * ct + 2 * l31);
      SCHED();
      const f32x4 s = (((t[0] + t[1]) + (t[2] + t[3])) + ((t[4] + t[5]) + (t[6] + t[7]))) +
                      (((t[8] + t[9]) + (t[10] + t[11])) + ((t[12] + t[13]) + (t[14] + t[15])));
      if (l31 < 20) {
        const unsigned q0 = pack2(s[0], s[1]), q1 = pack2(s[2], s[3]);
        *(LAS f32x4*)(skv + hf * 80 + l31 * 4) = (f32x4){bflo(q0), bfhi(q0), bflo(q1), bfhi(q1)};
      }
      asm volatile("s_waitcnt lgkmcnt(0)" ::: "memory");
      f32x4 o0 = {0.f, 0.f, 0.f, 0.f}, o1 = {0.f, 0.f, 0.f, 0.f};
#pragma unroll
      for (int e = 0; e < 20; ++e) {
        f32x4 kvv = *(LAS f32x4*)(skv + hf * 80 + e * 4);
        o0 += kvv * bflo(wv[e]);
        o1 += kvv * bfhi(wv[e]);
      }
      const auto rx = __builtin_amdgcn_permlane32_swap(pack2(o0[0], o0[1]), pack2(o1[0], o1[1]), false, false);
      const auto ry = __builtin_amdgcn_permlane32_swap(pack2(o0[2], o0[3]), pack2(o1[2], o1[3]), false, false);
      const u32x4 v = {rx[0], ry[0], rx[1], ry[1]};
      const int n = 64 * ct + 2 * l31 + hf;
      __builtin_amdgcn_raw_buffer_store_b128(v, wsr, (unsigned)(WS_BFRAG + ((size_t)(n >> 4) * NKS + 48 + ks) * 1024 + (16 * gg + (n & 15)) * 16), 0, 16);
    }
    SCHED();
    LOADZP();
#pragma unroll
    for (int tt = 0; tt < 5; ++tt)
#pragma unroll
      for (int mm = 0; mm < 4; ++mm) P[tt][mm] = LDF(a0 + (mm * NKS + tt) * 1024);
    SCHED();
    __builtin_amdgcn_s_setprio(APRIO);
    STAMP(1, 1);
    u32x4 Mf[3][5];
    const unsigned mo = (unsigned)(WS_BFRAG + ((size_t)(5 * nu) * NKS + 48 + 5 * aw) * 1024);
    u32x4 Z[5];
    {
      const f32x4 zs = (((zq[0] + zq[1]) + (zq[2] + zq[3])) + ((zq[4] + zq[5]) + (zq[6] + zq[7]))) +
                       (((zq[8] + zq[9]) + (zq[10] + zq[11])) + ((zq[12] + zq[13]) + (zq[14] + zq[15])));
      LAS char* zl = smem + w * 20480 + 1024;
      if (lane < 40) *(LAS u32x2*)(zl + (((lane >> 3) * 4 + (lane & 3)) * 2 + ((lane >> 2) & 1)) * 8) = (u32x2){pack2(zs[0], zs[1]), pack2(zs[2], zs[3])};
      asm volatile("s_waitcnt lgkmcnt(0)" ::: "memory");
#pragma unroll
      for (int t = 0; t < 5; ++t) Z[t] = *(LAS u32x4*)(zl + (t * 4 + g) * 16);
    }
    STAMP(1, 2);
    {
      f32x4 Dn[4][3];
#pragma unroll
      for (int sg = 0; sg < 3; ++sg) {
        const int hr = ((l15 >> 2) + 4 * (4 * sg + (l15 & 3))) / 5;
#pragma unroll
        for (int m = 0; m < 4; ++m) Dn[m][sg] = (f32x4){0.f, 0.f, 0.f, 0.f};
#pragma unroll
        for (int t = 0; t < 5; ++t) {
          const bool used = (sg == 0 && t <= 2) || (sg == 1 && t >= 1) || (sg == 2 && t >= 3);
          if (used) {
            const bool k0 = ((8 * t + g) / 5) == hr, k1 = ((8 * t + 4 + g) / 5) == hr;
            u32x4 v = {k0 ? Z[t][0] : 0u, k0 ? Z[t][1] : 0u, k1 ? Z[t][2] : 0u, k1 ? Z[t][3] : 0u};
            const bf16x8 zm = __builtin_bit_cast(bf16x8, v);
#pragma unroll
            for (int m = 0; m < 4; ++m) Dn[m][sg] = MFMA16(zm, P[t][m], Dn[m][sg]);
          }
        }
      }
#pragma unroll
      for (int m = 0; m < 4; ++m) {
#pragma unroll
        for (int t = 0; t < 5; ++t) {
          u32x4 p = __builtin_bit_cast(u32x4, P[t][m]);
          u32x4 q;
          const float r0 = __builtin_amdgcn_rcpf(Dn[m][(2 * t) >> 2][(2 * t) & 3] + 1e-6f);
          const float r1 = __builtin_amdgcn_rcpf(Dn[m][(2 * t + 1) >> 2][(2 * t + 1) & 3] + 1e-6f);
          q[0] = pack2(bflo(p[0]) * r0, bfhi(p[0]) * r0);
          q[1] = pack2(bflo(p[1]) * r0, bfhi(p[1]) * r0);
          q[2] = pack2(bflo(p[2]) * r1, bfhi(p[2]) * r1);
          q[3] = pack2(bflo(p[3]) * r1, bfhi(p[3]) * r1);
          P[t][m] = __builtin_bit_cast(bf16x8, q);
          asm volatile("" : "+v"(P[t][m]));
        }
      }
    }
    SCHED();
    STAMP(1, 6);
    STAMP(1, 7);
    __builtin_amdgcn_s_setprio(2);
#pragma unroll
    for (int t = 0; t < 3; ++t)
#pragma unroll
      for (int c = 0; c < 5; ++c) Mf[t][c] = __builtin_amdgcn_raw_buffer_load_b128(wsr, lv, mo + (c * NKS + t) * 1024, 16);
    SCHED();
    f32x4 acc[4][5];
    for (unsigned spins = 0;; ++spins) {
      unsigned mx = 0u;
#pragma unroll
      for (int t = 0; t < 3; ++t)
#pragma unroll
        for (int c = 0; c < 5; ++c) mx = max(max(mx, max(Mf[t][c][0], Mf[t][c][1])), max(Mf[t][c][2], Mf[t][c][3]));
      if (__builtin_expect(__all(mx != 0xffffffffu) || spins > (1u << 16), 1)) break;
      __builtin_amdgcn_s_sleep(32);
#pragma unroll
      for (int t = 0; t < 3; ++t)
#pragma unroll
        for (int c = 0; c < 5; ++c) Mf[t][c] = __builtin_amdgcn_raw_buffer_load_b128(wsr, lv, mo + (c * NKS + t) * 1024, 16);
    }
#pragma unroll
    for (int t = 0; t < 3; ++t) {
#pragma unroll
      for (int m = 0; m < 4; ++m)
#pragma unroll
        for (int c = 0; c < 5; ++c)
          acc[m][c] = MFMA16(__builtin_bit_cast(bf16x8, Mf[t][c]), P[t][m], t == 0 ? ((f32x4){0.f, 0.f, 0.f, 0.f}) : acc[m][c]);
      SCHED();
      if (t < 2) {
#pragma unroll
        for (int c = 0; c < 5; ++c) Mf[t][c] = __builtin_amdgcn_raw_buffer_load_b128(wsr, lv, mo + (c * NKS + t + 3) * 1024, 16);
      }
      SCHED();
    }
    for (unsigned spins = 0;; ++spins) {
      unsigned mx = 0u;
#pragma unroll
      for (int t = 0; t < 2; ++t)
#pragma unroll
        for (int c = 0; c < 5; ++c) mx = max(max(mx, max(Mf[t][c][0], Mf[t][c][1])), max(Mf[t][c][2], Mf[t][c][3]));
      if (__builtin_expect(__all(mx != 0xffffffffu) || spins > (1u << 16), 1)) break;
      __builtin_amdgcn_s_sleep(32);
#pragma unroll
      for (int t = 0; t < 2; ++t)
#pragma unroll
        for (int c = 0; c < 5; ++c) Mf[t][c] = __builtin_amdgcn_raw_buffer_load_b128(wsr, lv, mo + (c * NKS + t + 3) * 1024, 16);
    }
#pragma unroll
    for (int t = 3; t < 5; ++t) {
#pragma unroll
      for (int m = 0; m < 4; ++m)
#pragma unroll
        for (int c = 0; c < 5; ++c) acc[m][c] = MFMA16(__builtin_bit_cast(bf16x8, Mf[t - 3][c]), P[t][m], acc[m][c]);
    }
    STAMP(1, 3);
    LAS char* p = smem + w * 20480 + lane * 16;
#pragma unroll
    for (int m = 0; m < 4; ++m)
#pragma unroll
      for (int c = 0; c < 5; ++c) *(LAS f32x4*)(p + (m * 5 + c) * 1024) = acc[m][c];
  }
  const int fm = w >> 1, c0 = (w & 1) ? 3 : 0, nc = (w & 1) ? 2 : 3;
  const int row = 64 * rb + 16 * fm + l15;
  const float gv = ((const float*)(ws + WS_G))[row];
  u32x2 bb[3];
#pragma unroll
  for (int i = 0; i < 3; ++i) bb[i] = *(const u32x2*)(b2 + 80 * nu + 16 * (c0 + (i < nc ? i : 0)) + 4 * g);
  __syncthreads();
  STAMP(1, 4);
  {
    const float om = rbf(1.f - gv);
#pragma unroll
    for (int i = 0; i < 3; ++i) {
      if (i < nc) {
        const int c = c0 + i;
        LAS char* p = smem + (fm * 5 + c) * 1024 + lane * 16;
        f32x4 fb = (*(LAS f32x4*)(p) + *(LAS f32x4*)(p + 20480)) + (*(LAS f32x4*)(p + 2 * 20480) + *(LAS f32x4*)(p + 3 * 20480));
        f32x4 fa = (*(LAS f32x4*)(p + 4 * 20480) + *(LAS f32x4*)(p + 5 * 20480)) + (*(LAS f32x4*)(p + 6 * 20480) + *(LAS f32x4*)(p + 7 * 20480));
        const int n = 80 * nu + 16 * c + 4 * g;
        const unsigned b01 = pack2(fb[0], fb[1]), b23 = pack2(fb[2], fb[3]);
        const unsigned y01 = pack2(bflo(b01) + bflo(bb[i][0]), bfhi(b01) + bfhi(bb[i][0]));
        const unsigned y23 = pack2(bflo(b23) + bflo(bb[i][1]), bfhi(b23) + bfhi(bb[i][1]));
        const unsigned a01 = pack2(fa[0], fa[1]), a23 = pack2(fa[2], fa[3]);
        const unsigned ga01 = pack2(gv * bflo(a01), gv * bfhi(a01)), ga23 = pack2(gv * bflo(a23), gv * bfhi(a23));
        const unsigned gb01 = pack2(om * bflo(y01), om * bfhi(y01)), gb23 = pack2(om * bflo(y23), om * bfhi(y23));
        u32x2 v = {pack2(bflo(ga01) + bflo(gb01), bfhi(ga01) + bfhi(gb01)), pack2(bflo(ga23) + bflo(gb23), bfhi(ga23) + bfhi(gb23))};
        *(u32x2*)(out + (size_t)row * D_ + n) = v;
      }
    }
  }
  STAMP(1, 5);
}

extern "C" void kernel_launch(void* const* d_in, const int* in_sizes, int n_in, void* d_out, int out_size,
                              void* d_ws, size_t ws_size, hipStream_t stream) {
  const u16* x = (const u16*)d_in[0];
  const u16* wqkv = (const u16*)d_in[1];
  const u16* wo = (const u16*)d_in[2];
  const u16* w1 = (const u16*)d_in[4];
  const u16* b1 = (const u16*)d_in[5];
  const u16* w2 = (const u16*)d_in[6];
  const u16* b2 = (const u16*)d_in[7];
  const u16* gw = (const u16*)d_in[8];
  char* ws = (char*)d_ws;
  (void)hipFuncSetAttribute((const void*)k1_kernel, hipFuncAttributeMaxDynamicSharedMemorySize, K1_LDS);
  (void)hipFuncSetAttribute((const void*)k2_kernel, hipFuncAttributeMaxDynamicSharedMemorySize, K2_LDS);
  k1_kernel<<<256, 512, K1_LDS, stream>>>(x, wqkv, w1, b1, w2, gw, ws);
  k2_kernel<<<256, 512, K2_LDS, stream>>>(wo, b2, ws, (u16*)d_out);
}
```

```cpp
#include <hip/hip_runtime.h>
#include <hip/hip_bf16.h>
#include <stdint.h>

typedef __attribute__((ext_vector_type(8))) short bf16x8;
typedef __attribute__((ext_vector_type(4))) short s16x4;
typedef __attribute__((ext_vector_type(2))) short s16x2;
typedef __attribute__((ext_vector_type(4))) float f32x4;
typedef __attribute__((ext_vector_type(2))) float f32x2;
typedef __attribute__((ext_vector_type(2))) __bf16 bf16x2v;
typedef __attribute__((ext_vector_type(4))) unsigned u32x4;
typedef __attribute__((ext_vector_type(2))) unsigned u32x2;
typedef unsigned short u16;
typedef unsigned long long u64;
#define LAS __attribute__((address_space(3)))
#define GAS __attribute__((address_space(1)))
#define DI __device__ __forceinline__

constexpr int S_ = 2048, D_ = 640, F_ = 1536;
constexpr int NKS = 68;

constexpr unsigned WS_EPOCH = 0;
constexpr unsigned WS_F1 = 1024;
constexpr unsigned WS_FK = 12288;
constexpr unsigned WS_FG = 8192;
constexpr unsigned WS_F0 = 4096;
constexpr unsigned WS_AFRAG = 16384;
constexpr unsigned WS_MFRAG = WS_AFRAG + 128 * NKS * 1024;
constexpr unsigned WS_W2F = WS_MFRAG + 40 * 20 * 1024;
constexpr unsigned WS_KVP = WS_W2F + 40 * 48 * 1024;
constexpr unsigned WS_ZP = WS_KVP + 16 * 51200;
constexpr unsigned WS_G = WS_ZP + 16 * 2560;
constexpr unsigned WS_END = WS_G + 2048 * 4;

DI unsigned pack2(float a, float b) {
  f32x2 f = {a, b};
  return __builtin_bit_cast(unsigned, __builtin_convertvector(f, bf16x2v));
}
DI float rbf(float a) { return __uint_as_float(pack2(a, a) << 16); }
DI float bflo(unsigned u) { return __uint_as_float(u << 16); }
DI float bfhi(unsigned u) { return __uint_as_float(u & 0xffff0000u); }
DI unsigned phi2(float a0, float a1) {
  const unsigned p = pack2(a0, a1);
  const float t0 = bflo(p), t1 = bfhi(p);
  const float e0 = __builtin_amdgcn_exp2f(fminf(t0 * 1.44269504f, 0.f));
  const float e1 = __builtin_amdgcn_exp2f(fminf(t1 * 1.44269504f, 0.f));
  return pack2((fmaxf(t0, 0.f) + e0) + 1e-6f, (fmaxf(t1, 0.f) + e1) + 1e-6f);
}
DI f32x4 sel4(bool c, f32x4 a, f32x4 b) {
  f32x4 r;
#pragma unroll
  for (int i = 0; i < 4; ++i) { float x = a[i], y = b[i]; asm volatile("" : "+v"(x), "+v"(y)); r[i] = c ? x : y; }
  return r;
}
DI unsigned sqrelu2(float a0, float a1) {
  unsigned q = pack2(a0, a1);
  const s16x2 z = {0, 0};
  q = __builtin_bit_cast(unsigned, __builtin_elementwise_max(__builtin_bit_cast(s16x2, q), z));
  const float u0 = bflo(q), u1 = bfhi(q);
  return pack2(u0 * u0, u1 * u1);
}
#define STAMP(kern,i)
#define MFMA16(a, b, c) __builtin_amdgcn_mfma_f32_16x16x32_bf16((a), (b), (c), 0, 0, 0)
#define SCHED() __builtin_amdgcn_sched_barrier(0)
#define SGB(mask, n) __builtin_amdgcn_sched_group_barrier(mask, n, 0)
#define INTERLEAVE() do { _Pragma("unroll") for (int q_ = 0; q_ < 18; ++q_) { SGB(0x100, 1); SGB(0x8, 1); } SGB(0x8, 10); } while (0)
#define BAR() do { asm volatile("s_waitcnt lgkmcnt(0)" ::: "memory"); __builtin_amdgcn_s_barrier(); asm volatile("" ::: "memory"); } while (0)
#define DRAIN() asm volatile("s_waitcnt vmcnt(0)" ::: "memory")
#define ST16(off, v) __builtin_amdgcn_raw_buffer_store_b128(__builtin_bit_cast(u32x4, v), wsr, (unsigned)(off), 0, 16)
#define ST8(off, v) __builtin_amdgcn_raw_buffer_store_b64((v), wsr, (unsigned)(off), 0, 16)
#define LD16(voff, soff) __builtin_amdgcn_raw_buffer_load_b128(wsr, (unsigned)(voff), (unsigned)(soff), 16)
constexpr unsigned SPIN_CAP = 1u << 18;
#define TOUCH_AT 10
#define DO_TOUCH (TOUCH_AT < 10)
#define PROBE_SLEEP 8
#define POLL_SLEEP 8

constexpr int K1_XB = 16384;
constexpr int K1_WBLK = 1056;
constexpr int K1_WGRP = 8 * K1_WBLK;
constexpr int K1_WB = 32 * K1_WBLK;
constexpr int K1_STAGE = K1_XB + K1_WB;
constexpr int RING1 = 81920;
constexpr int KVRED = 61440;
constexpr int FSCR = 143360;
constexpr int ASCR = 150528;
constexpr int CTRL = 162816;
constexpr int LDS_BYTES = 163840;

DI void dma16(unsigned lds_off, unsigned voff, const char* gbase) {
  const unsigned lo = __builtin_amdgcn_readfirstlane((unsigned)(uintptr_t)gbase);
  const unsigned hi = __builtin_amdgcn_readfirstlane((unsigned)((uintptr_t)gbase >> 32));
  const u64 gb = ((u64)hi << 32) | lo;
  const unsigned lo2 = __builtin_amdgcn_readfirstlane(lds_off);
  asm volatile("s_mov_b32 m0, %0\n\ts_nop 3\n\tglobal_load_lds_dwordx4 %1, %2"
               :: "s"(lo2), "v"(voff), "s"(gb) : "memory");
}

DI void dma16_sc1(unsigned lds_off, unsigned voff, const char* gbase) {
  const unsigned lo = __builtin_amdgcn_readfirstlane((unsigned)(uintptr_t)gbase);
  const unsigned hi = __builtin_amdgcn_readfirstlane((unsigned)((uintptr_t)gbase >> 32));
  const u64 gb = ((u64)hi << 32) | lo;
  const unsigned lo2 = __builtin_amdgcn_readfirstlane(lds_off);
  asm volatile("s_mov_b32 m0, %0\n\ts_nop 3\n\tglobal_load_lds_dwordx4 %1, %2 sc1"
               :: "s"(lo2), "v"(voff), "s"(gb) : "memory");
}

DI u64 load_tag(const char* ws) {
  u64 e;
  asm volatile("s_load_dwordx2 %0, %1, 0x0\n\ts_waitcnt lgkmcnt(0)" : "=s"(e) : "s"(ws) : "memory");
  return e + 1ull;
}
DI bf16x8 tr_frag(LAS char* p0, int off, int step) {
  s16x4 lo = __builtin_amdgcn_ds_read_tr16_b64_v4i16((LAS s16x4*)(p0 + off));
  s16x4 hi = __builtin_amdgcn_ds_read_tr16_b64_v4i16((LAS s16x4*)(p0 + off + step));
  return __builtin_shufflevector(lo, hi, 0, 1, 2, 3, 4, 5, 6, 7);
}

DI void wait_flags(char* ws, unsigned off, bool care, u64 tag) {
  GAS u64* p = (GAS u64*)(ws + off);
  for (unsigned spins = 0;; ++spins) {
    const u64 v = __hip_atomic_load(p, __ATOMIC_RELAXED, __HIP_MEMORY_SCOPE_AGENT);
    if (__all(!care || v == tag) || spins > SPIN_CAP) break;
    __builtin_amdgcn_s_sleep(POLL_SLEEP);
  }
  __builtin_amdgcn_fence(__ATOMIC_ACQUIRE, "wavefront");
  asm volatile("" ::: "memory");
}

DI void lds_signal(LAS char* smem, int word, int lane) {
  asm volatile("s_waitcnt lgkmcnt(0)" ::: "memory");
  if (lane == 0) __hip_atomic_fetch_add((LAS unsigned*)(smem + CTRL) + word, 1u, __ATOMIC_RELAXED, __HIP_MEMORY_SCOPE_WORKGROUP);
}
DI void lds_wait(LAS char* smem, int word, unsigned n) {
  while (__builtin_amdgcn_readfirstlane(__hip_atomic_load((LAS unsigned*)(smem + CTRL) + word, __ATOMIC_RELAXED, __HIP_MEMORY_SCOPE_WORKGROUP)) < n)
    __builtin_amdgcn_s_sleep(1);
  asm volatile("" ::: "memory");
}
DI void arrive_publish(LAS char* smem, int word, char* ws, unsigned flag_off, u64 tag, unsigned n, int lane) {
  if (lane == 0) {
    const unsigned old = __hip_atomic_fetch_add((LAS unsigned*)(smem + CTRL) + word, 1u, __ATOMIC_RELAXED, __HIP_MEMORY_SCOPE_WORKGROUP);
    if (old == n - 1) {
      __hip_atomic_store((GAS u64*)(ws + flag_off), tag, __ATOMIC_RELAXED, __HIP_MEMORY_SCOPE_AGENT);
    }
  }
}

template <bool KV>
DI void p1_compute(const u32x2 (&bb)[7], char* ws, __amdgpu_buffer_rsrc_t wsr, LAS char* smem, int rb, int cb, int w, u64 tag) {
  const int lane = threadIdx.x & 63;
  const int wr = w >> 1, wc = w & 1;
  const int g = lane >> 4, l15 = lane & 15;

  const int xrd = (wr * 64 + l15) * 128;
  const int xsw = l15 & 7;
  const int wrow = K1_XB + (4 * (g & 1) + (l15 >> 2)) * K1_WBLK + (g >> 1) * 512;
  const int ka = 32 * (cb & 1);
  const int wrd = wrow + (KV ? ka + wc * 80 : wc * 224) + (l15 & 3) * 8;
  const int wrdq = wrow + 384 + wc * 64 + (l15 & 3) * 8;
  const int wrd4 = wrow + ka + wc * 80 + 32 + (l15 & 1) * 40 + ((l15 >> 1) & 1) * 192;

  f32x4 acc[7][4];
  bf16x8 a0[4], b0[7], a1[4], b1f[7];
  auto rd = [&](LAS char* sb, int s, bf16x8 (&a)[4], bf16x8 (&b)[7]) {
#pragma unroll
    for (int m = 0; m < 4; ++m) a[m] = *(LAS bf16x8*)(sb + xrd + m * 2048 + (((4 * s + g) ^ xsw) << 4));
    if (KV) {
      b[0] = tr_frag(sb + wrd, s * 2 * K1_WGRP + 0, K1_WGRP);
      b[1] = tr_frag(sb + wrd, s * 2 * K1_WGRP + 40, K1_WGRP);
      b[2] = tr_frag(sb + wrd, s * 2 * K1_WGRP + 192, K1_WGRP);
      b[3] = tr_frag(sb + wrd, s * 2 * K1_WGRP + 232, K1_WGRP);
      b[4] = tr_frag(sb + wrd4, s * 2 * K1_WGRP, K1_WGRP);
      b[5] = tr_frag(sb + wrdq, s * 2 * K1_WGRP, K1_WGRP);
      b[6] = tr_frag(sb + wrdq, s * 2 * K1_WGRP + 32, K1_WGRP);
    } else {
#pragma unroll
      for (int t = 0; t < 7; ++t) b[t] = tr_frag(sb + wrd, s * 2 * K1_WGRP + 32 * t, K1_WGRP);
    }
  };
  auto mm = [&](bf16x8 (&a)[4], bf16x8 (&b)[7]) {
#pragma unroll
    for (int t = 0; t < 7; ++t)
#pragma unroll
      for (int m = 0; m < 4; ++m) {
        if (KV && t < 5) acc[t][m] = MFMA16(a[m], b[t], acc[t][m]);
        else acc[t][m] = MFMA16(b[t], a[m], acc[t][m]);
      }
  };
  BAR();
  rd(smem, 0, a0, b0);
  rd(smem, 1, a1, b1f);

#pragma unroll
  for (int t = 0; t < 7; ++t) {
    f32x4 init = {0.f, 0.f, 0.f, 0.f};
    if (!KV && cb > 8) init = (f32x4){bflo(bb[t][0]), bfhi(bb[t][0]), bflo(bb[t][1]), bfhi(bb[t][1])};
#pragma unroll
    for (int m = 0; m < 4; ++m) acc[t][m] = init;
  }
  mm(a0, b0);
  int buf = 1;
  auto iter = [&]() {
    BAR();
    LAS char* sb = smem + buf * K1_STAGE;
    SCHED();
    rd(sb, 0, a0, b0);
    mm(a1, b1f);
    INTERLEAVE();
    SCHED();
    rd(sb, 1, a1, b1f);
    mm(a0, b0);
    INTERLEAVE();
    SCHED();
    buf = buf + 1; if (buf == 3) buf = 0;
  };
#pragma unroll 1
  for (int i = 1; i < TOUCH_AT; ++i) iter();
  unsigned t0_ = 0, t1_ = 0;
  if (DO_TOUCH && __builtin_amdgcn_readfirstlane(__hip_atomic_load((LAS unsigned*)(smem + CTRL) + 9, __ATOMIC_RELAXED, __HIP_MEMORY_SCOPE_WORKGROUP)) != 0u) {
    const unsigned ln = ((blockIdx.x >> 3) * 4 + w) * 64 + lane;
    t0_ = __builtin_amdgcn_raw_buffer_load_b32(wsr, WS_W2F + ln * 128, 0, 16);
    t1_ = __builtin_amdgcn_raw_buffer_load_b32(wsr, WS_W2F + (ln < 7168 ? ln + 8192 : ln) * 128, 0, 16);
  }
#pragma unroll 1
  for (int i = TOUCH_AT; i < 10; ++i) iter();
  asm volatile("" :: "v"(t0_), "v"(t1_));
  lds_signal(smem, 2, lane);
  __builtin_amdgcn_s_setprio(3);
  mm(a1, b1f);
  STAMP(0, 3);

  const int rt0 = rb * 8 + wr * 4;
  if (KV) {
    const int hp = 2 * cb + wc;
#pragma unroll
    for (int m = 0; m < 4; ++m) {
      u32x4 v;
      v[0] = phi2(acc[5][m][0], acc[5][m][1]);
      v[1] = phi2(acc[5][m][2], acc[5][m][3]);
      v[2] = phi2(acc[6][m][0], acc[6][m][1]);
      v[3] = phi2(acc[6][m][2], acc[6][m][3]);
      ST16(WS_AFRAG + ((rt0 + m) * NKS + 48 + hp) * 1024 + lane * 16, v);
    }
    SCHED();
    f32x4 R[10];
#pragma unroll
    for (int t = 0; t < 10; ++t) R[t] = (f32x4){0.f, 0.f, 0.f, 0.f};
    const bool iskl = l15 < 8;
    const bf16x8 ones = {0x3f80, 0x3f80, 0x3f80, 0x3f80, 0x3f80, 0x3f80, 0x3f80, 0x3f80};
#pragma unroll
    for (int u = 0; u < 2; ++u) {
      bf16x8 T[5];
#pragma unroll
      for (int t = 0; t < 5; ++t) {
        u32x4 p;
#pragma unroll
        for (int j = 0; j < 4; ++j) {
          const float e0 = acc[t][2 * u + (j >> 1)][2 * (j & 1)], e1 = acc[t][2 * u + (j >> 1)][2 * (j & 1) + 1];
          if (t < 2) p[j] = phi2(e0, e1);
          else if (t < 4) p[j] = pack2(e0, e1);
          else p[j] = iskl ? phi2(e0, e1) : pack2(e0, e1);
        }
        T[t] = __builtin_bit_cast(bf16x8, p);
      }
      R[0] = MFMA16(T[0], T[2], R[0]);
      R[1] = MFMA16(T[0], T[4], R[1]);
      R[2] = MFMA16(T[4], T[2], R[2]);
      R[3] = MFMA16(T[1], T[3], R[3]);
      R[4] = MFMA16(T[1], T[4], R[4]);
      R[5] = MFMA16(T[4], T[3], R[5]);
      R[6] = MFMA16(T[4], T[4], R[6]);
      R[7] = MFMA16(T[0], ones, R[7]);
      R[8] = MFMA16(T[1], ones, R[8]);
      R[9] = MFMA16(T[4], ones, R[9]);
    }
    LAS char* red = smem + KVRED + wc * 10240;
    if (wr == 1) {
#pragma unroll
      for (int t = 0; t < 10; ++t) *(LAS f32x4*)(red + t * 1024 + lane * 16) = R[t];
      lds_signal(smem, 3 + wc, lane);
    }
    lds_signal(smem, 7, lane);
    DRAIN();
    arrive_publish(smem, 0, ws, WS_F1 + (rb * 16 + cb) * 8, tag, 4, lane);
    if (wr == 0) {
      lds_wait(smem, 3 + wc, 1);
#pragma unroll
      for (int t = 0; t < 10; ++t) R[t] += *(LAS f32x4*)(red + t * 1024 + lane * 16);
      const unsigned kp = WS_KVP + rb * 51200 + (10 * hp) * 320;
      ST16(kp + g * 320 + l15 * 16, R[0]);
      ST16(kp + 1600 + g * 320 + l15 * 16, R[3]);
      if (l15 >= 8) ST16(kp + (l15 >= 12 ? 1600 : 0) + g * 320 + (16 + (l15 & 3)) * 16, sel4(l15 >= 12, R[4], R[1]));
      if (g < 2) ST16(kp + g * 1600 + 4 * 320 + l15 * 16, sel4(g != 0, R[5], R[2]));
      if (g < 2 && (l15 >> 2) == 2 + g) ST16(kp + g * 1600 + 4 * 320 + (16 + (l15 & 3)) * 16, R[6]);
      const unsigned zp = WS_ZP + rb * 2560 + (10 * hp) * 16;
      if (l15 < 2 || (l15 == 2 && g < 2))
        ST16(zp + (l15 == 0 ? g * 16 : (l15 == 1 ? 80 + g * 16 : g * 80 + 64)), sel4(l15 == 0, R[7], sel4(l15 == 1, R[8], R[9])));
    }
  } else {
    if (cb == 8) {
#pragma unroll
      for (int pp = 0; pp < 2; ++pp)
#pragma unroll
        for (int m = 0; m < 4; ++m) {
          u32x4 v;
          v[0] = phi2(acc[2 * pp][m][0], acc[2 * pp][m][1]);
          v[1] = phi2(acc[2 * pp][m][2], acc[2 * pp][m][3]);
          v[2] = phi2(acc[2 * pp + 1][m][0], acc[2 * pp + 1][m][1]);
          v[3] = phi2(acc[2 * pp + 1][m][2], acc[2 * pp + 1][m][3]);
          ST16(WS_AFRAG + ((rt0 + m) * NKS + 48 + 16 + 2 * wc + pp) * 1024 + lane * 16, v);
        }
    } else {
      const int T0 = 14 * (cb - 9) + 7 * wc;
      const unsigned base = WS_AFRAG + (rt0 * NKS + (T0 >> 1)) * 1024 + lane * 16;
      const bool full = T0 + 6 < 96;
#pragma unroll
      for (int m = 0; m < 4; ++m) {
        u32x2 hv[7];
#pragma unroll
        for (int t = 0; t < 7; ++t) {
          hv[t][0] = sqrelu2(acc[t][m][0], acc[t][m][1]);
          hv[t][1] = sqrelu2(acc[t][m][2], acc[t][m][3]);
        }
        const unsigned bm = base + m * NKS * 1024;
        if (wc == 0) {
#pragma unroll
          for (int pp = 0; pp < 3; ++pp) {
            u32x4 v = {hv[2 * pp][0], hv[2 * pp][1], hv[2 * pp + 1][0], hv[2 * pp + 1][1]};
            ST16(bm + pp * 1024, v);
          }
          ST8(bm + 3 * 1024, hv[6]);
        } else {
          ST8(bm + 8, hv[0]);
#pragma unroll
          for (int pp = 0; pp < 3; ++pp) {
            u32x4 v = {hv[2 * pp + 1][0], hv[2 * pp + 1][1], hv[2 * pp + 2][0], hv[2 * pp + 2][1]};
            if (pp < 2 || full) ST16(bm + (pp + 1) * 1024, v);
          }
        }
      }
    }
    lds_signal(smem, 7, lane);
    DRAIN();
    arrive_publish(smem, 0, ws, WS_F1 + (rb * 16 + cb) * 8, tag, 4, lane);
  }
}

#define WINF 4
constexpr int WIN = WINF;
static_assert(WIN <= 6, "ring fills must be older than the h loads of the same step");
constexpr int fops(int it) { return (it < 3 ? 5 : 0) + (it + WIN < 12 ? 4 : 0); }
constexpr int nwait(int s) {
  int n = 0;
  if (s < WIN) { n += 4 * (WIN - 1 - s); for (int it = 0; it < s; ++it) n += fops(it); }
  else { for (int it = s - WIN + 1; it < s; ++it) n += fops(it); }
  return n;
}
template <int N> DI void waitv() { asm volatile("s_waitcnt vmcnt(%0)" :: "n"(N) : "memory"); }

__global__ void __launch_bounds__(512, 2)
fused_kernel(const u16* __restrict__ x, const u16* __restrict__ wqkv, const u16* __restrict__ wo,
             const u16* __restrict__ w1, const u16* __restrict__ b1, const u16* __restrict__ w2,
             const u16* __restrict__ b2, const u16* __restrict__ gate_w, char* ws, u16* __restrict__ out) {
  extern __shared__ __attribute__((aligned(16))) char smem_[];
  LAS char* smem = (LAS char*)smem_;
  const int bid = blockIdx.x, tid = threadIdx.x, lane = tid & 63;
  const int w = __builtin_amdgcn_readfirstlane(tid >> 6);
  const int g = lane >> 4, l15 = lane & 15;
  const int xcd = bid & 7, idx = bid >> 3;
  const int rb = 8 * (xcd >> 2) + (idx >> 2);
  const int cb = (xcd & 3) + 4 * (idx & 3);
  const int rb2 = 4 * xcd + (idx >> 3);
  const int nu = idx & 7;
  const __amdgpu_buffer_rsrc_t wsr = __builtin_amdgcn_make_buffer_rsrc(ws, 0, (int)WS_END, 0x00020000);
  const unsigned lv = lane * 16;
  const unsigned ap = WS_AFRAG + (4 * rb2) * NKS * 1024;
  const unsigned lds0 = (unsigned)(size_t)smem;
  STAMP(0, 0);
  f32x4 acc[4][5];
  u64 tagf = 0;

  if (w < 4) {
    if (tid < 16) ((LAS unsigned*)(smem + CTRL))[tid] = 0u;
    u32x2 bb[7];
#pragma unroll
    for (int t = 0; t < 7; ++t) {
      int T = 14 * (cb - 9) + 7 * (w & 1) + t;
      T = T < 0 ? 0 : (T > 95 ? 95 : T);
      bb[t] = *(const u32x2*)(b1 + 16 * T + 4 * g);
    }
    const int aw = w;
    const u64 tag = load_tag(ws);
    if (cb < 8) p1_compute<true>(bb, ws, wsr, smem, rb, cb, w, tag);
    else p1_compute<false>(bb, ws, wsr, smem, rb, cb, w, tag);
    STAMP(0, 4);
    __builtin_amdgcn_s_setprio(1);
    STAMP(0, 5);
    lds_wait(smem, 1, 1);
#define F4(x) __builtin_bit_cast(f32x4, x)
    STAMP(0, 6);
    const int J = 32 * aw + idx;
    const int ks = xcd + 8 * (J / 40);
    const bool job = J < 120 && ks < 20;
    const int gg = (J % 40) / 10, ct = J % 10;
    const int hf = lane >> 5, l31 = lane & 31;
    const unsigned mst = WS_MFRAG + (((64 * ct + 2 * l31 + hf) >> 4) * 20 + ks) * 1024 + (16 * gg + ((2 * l31 + hf) & 15)) * 16;
    const int pm = 8 * ks + 4 * hf + gg, ph = pm / 5;
    unsigned wv[20];
    if (job) {
#pragma unroll
      for (int e = 0; e < 20; ++e) wv[e] = *(const unsigned*)(wo + (size_t)(20 * ph + e) * D_ + 64 * ct + 2 * l31);
    }
    {
      const int pr = 5 * aw + (lane < 5 ? lane : 0);
      wait_flags(ws, WS_F1 + ((rb2 >> 1) * 16 + (pr < 16 ? pr >> 1 : 8)) * 8, true, tag);
    }
    bf16x8 P[5][4];
    u32x4 Z[5];
    {
      const unsigned a0 = ap + (48 + 5 * aw) * 1024;
#pragma unroll
      for (int tt = 0; tt < 5; ++tt)
#pragma unroll
        for (int mm = 0; mm < 4; ++mm) P[tt][mm] = __builtin_bit_cast(bf16x8, LD16(lv, a0 + (mm * NKS + tt) * 1024));
      SCHED();
      __builtin_amdgcn_s_setprio(2);
      {
        const unsigned po = lane < 32 ? (job ? WS_KVP + (lane & 15) * 51200 + (8 * ks + 4 * (lane >> 4) + gg) * 320
                                             : WS_ZP + (lane & 15) * 2560 + (40 * aw + 10 * (lane >> 4)) * 16)
                                      : WS_ZP + (lane & 15) * 2560 + (40 * aw + 20 + 10 * ((lane >> 4) & 1)) * 16;
        for (unsigned spins = 0;; ++spins) {
          const unsigned v = __builtin_amdgcn_raw_buffer_load_b32(wsr, po, 0, 16);
          if (__all(v != 0xffffffffu) || spins > SPIN_CAP) break;
          __builtin_amdgcn_s_sleep(PROBE_SLEEP);
        }
      }
      u32x4 zq[16], t[16];
      const unsigned zo = (unsigned)((40 * aw + (lane < 40 ? lane : 39)) * 16);
      const unsigned kvo = (unsigned)((job ? pm : 0) * 320 + (l31 < 20 ? l31 : 19) * 16);
      for (unsigned spins = 0;; ++spins) {
#pragma unroll
        for (int r = 0; r < 16; ++r) t[r] = LD16(kvo, WS_KVP + r * 51200);
#pragma unroll
        for (int r = 0; r < 16; ++r) zq[r] = LD16(zo, WS_ZP + r * 2560);
        unsigned mx = 0u;
#pragma unroll
        for (int r = 0; r < 16; ++r) mx = max(max(mx, max(t[r][0], t[r][1])), max(t[r][2], t[r][3]));
#pragma unroll
        for (int r = 0; r < 16; ++r) mx = max(max(mx, max(zq[r][0], zq[r][1])), max(zq[r][2], zq[r][3]));
        if (__builtin_expect(__all(mx != 0xffffffffu) || spins > SPIN_CAP, 1)) break;
        __builtin_amdgcn_s_sleep(PROBE_SLEEP);
      }
      if (job) {
        LAS float* skv = (LAS float*)(smem + ASCR + w * 3072);
        const f32x4 s = (((F4(t[0]) + F4(t[1])) + (F4(t[2]) + F4(t[3]))) + ((F4(t[4]) + F4(t[5])) + (F4(t[6]) + F4(t[7])))) +
                        (((F4(t[8]) + F4(t[9])) + (F4(t[10]) + F4(t[11]))) + ((F4(t[12]) + F4(t[13])) + (F4(t[14]) + F4(t[15]))));
        if (l31 < 20) {
          const unsigned q0 = pack2(s[0], s[1]), q1 = pack2(s[2], s[3]);
          *(LAS f32x4*)(skv + hf * 80 + l31 * 4) = (f32x4){bflo(q0), bfhi(q0), bflo(q1), bfhi(q1)};
        }
        asm volatile("s_waitcnt lgkmcnt(0)" ::: "memory");
        f32x4 o0 = {0.f, 0.f, 0.f, 0.f}, o1 = {0.f, 0.f, 0.f, 0.f};
#pragma unroll
        for (int e = 0; e < 20; ++e) {
          f32x4 kvv = *(LAS f32x4*)(skv + hf * 80 + e * 4);
          o0 += kvv * bflo(wv[e]);
          o1 += kvv * bfhi(wv[e]);
        }
        const auto rx = __builtin_amdgcn_permlane32_swap(pack2(o0[0], o0[1]), pack2(o1[0], o1[1]), false, false);
        const auto ry = __builtin_amdgcn_permlane32_swap(pack2(o0[2], o0[3]), pack2(o1[2], o1[3]), false, false);
        const u32x4 v = {rx[0], ry[0], rx[1], ry[1]};
        ST16(mst, v);
      }
      SCHED();
      const f32x4 zs = (((F4(zq[0]) + F4(zq[1])) + (F4(zq[2]) + F4(zq[3]))) + ((F4(zq[4]) + F4(zq[5])) + (F4(zq[6]) + F4(zq[7])))) +
                       (((F4(zq[8]) + F4(zq[9])) + (F4(zq[10]) + F4(zq[11]))) + ((F4(zq[12]) + F4(zq[13])) + (F4(zq[14]) + F4(zq[15]))));
      LAS char* zl = smem + ASCR + w * 3072 + 1024;
      if (lane < 40) *(LAS u32x2*)(zl + (((lane >> 3) * 4 + (lane & 3)) * 2 + ((lane >> 2) & 1)) * 8) = (u32x2){pack2(zs[0], zs[1]), pack2(zs[2], zs[3])};
      asm volatile("s_waitcnt lgkmcnt(0)" ::: "memory");
#pragma unroll
      for (int t = 0; t < 5; ++t) Z[t] = *(LAS u32x4*)(zl + (t * 4 + g) * 16);
    }
    u32x4 Mf[3][5];
    const unsigned mo = WS_MFRAG + ((5 * nu) * 20 + 5 * aw) * 1024;
    {
      f32x4 Dn[4][3];
#pragma unroll
      for (int sg = 0; sg < 3; ++sg) {
        const int hr = ((l15 >> 2) + 4 * (4 * sg + (l15 & 3))) / 5;
#pragma unroll
        for (int m = 0; m < 4; ++m) Dn[m][sg] = (f32x4){0.f, 0.f, 0.f, 0.f};
#pragma unroll
        for (int t = 0; t < 5; ++t) {
          const bool used = (sg == 0 && t <= 2) || (sg == 1 && t >= 1) || (sg == 2 && t >= 3);
          if (used) {
            const bool k0 = ((8 * t + g) / 5) == hr, k1 = ((8 * t + 4 + g) / 5) == hr;
            u32x4 v = {k0 ? Z[t][0] : 0u, k0 ? Z[t][1] : 0u, k1 ? Z[t][2] : 0u, k1 ? Z[t][3] : 0u};
            const bf16x8 zm = __builtin_bit_cast(bf16x8, v);
#pragma unroll
            for (int m = 0; m < 4; ++m) Dn[m][sg] = MFMA16(zm, P[t][m], Dn[m][sg]);
          }
        }
      }
#pragma unroll
      for (int m = 0; m < 4; ++m) {
#pragma unroll
        for (int t = 0; t < 5; ++t) {
          u32x4 p = __builtin_bit_cast(u32x4, P[t][m]);
          u32x4 q;
          const float r0 = __builtin_amdgcn_rcpf(Dn[m][(2 * t) >> 2][(2 * t) & 3] + 1e-6f);
          const float r1 = __builtin_amdgcn_rcpf(Dn[m][(2 * t + 1) >> 2][(2 * t + 1) & 3] + 1e-6f);
          q[0] = pack2(bflo(p[0]) * r0, bfhi(p[0]) * r0);
          q[1] = pack2(bflo(p[1]) * r0, bfhi(p[1]) * r0);
          q[2] = pack2(bflo(p[2]) * r1, bfhi(p[2]) * r1);
          q[3] = pack2(bflo(p[3]) * r1, bfhi(p[3]) * r1);
          P[t][m] = __builtin_bit_cast(bf16x8, q);
          asm volatile("" : "+v"(P[t][m]));
        }
      }
    }
    SCHED();
    STAMP(0, 7);
    {
      STAMP(1, 1);
#pragma unroll
      for (int t = 0; t < 3; ++t)
#pragma unroll
        for (int c = 0; c < 5; ++c) Mf[t][c] = LD16(lv, mo + (c * 20 + t) * 1024);
      for (unsigned spins = 0;; ++spins) {
        unsigned mx = 0u;
#pragma unroll
        for (int t = 0; t < 3; ++t)
#pragma unroll
          for (int c = 0; c < 5; ++c) mx = max(max(mx, max(Mf[t][c][0], Mf[t][c][1])), max(Mf[t][c][2], Mf[t][c][3]));
        if (__builtin_expect(__all(mx != 0xffffffffu) || spins > SPIN_CAP, 1)) break;
        const int pl = lane < 40 ? lane : 39, pt = pl >> 3, pg = (pl >> 1) & 3, pe = pl & 1;
        const unsigned po = mo + ((pe ? 4 : 0) * 20 + pt) * 1024 + (16 * pg + (pe ? 15 : 0)) * 16;
        for (unsigned sp2 = 0;; ++sp2) {
          const unsigned v = __builtin_amdgcn_raw_buffer_load_b32(wsr, po, 0, 16);
          if (__all(v != 0xffffffffu) || sp2 > SPIN_CAP) break;
          __builtin_amdgcn_s_sleep(24);
        }
#pragma unroll
        for (int t = 0; t < 3; ++t)
#pragma unroll
          for (int c = 0; c < 5; ++c) Mf[t][c] = LD16(lv, mo + (c * 20 + t) * 1024);
      }
#pragma unroll
      for (int t = 0; t < 3; ++t) {
#pragma unroll
        for (int mm = 0; mm < 4; ++mm)
#pragma unroll
          for (int c = 0; c < 5; ++c)
            acc[mm][c] = MFMA16(__builtin_bit_cast(bf16x8, Mf[t][c]), P[t][mm], t == 0 ? ((f32x4){0.f, 0.f, 0.f, 0.f}) : acc[mm][c]);
        SCHED();
        if (t < 2) {
#pragma unroll
          for (int c = 0; c < 5; ++c) Mf[t][c] = LD16(lv, mo + (c * 20 + t + 3) * 1024);
        }
        SCHED();
      }
      for (unsigned spins = 0;; ++spins) {
        unsigned mx = 0u;
#pragma unroll
        for (int t = 0; t < 2; ++t)
#pragma unroll
          for (int c = 0; c < 5; ++c) mx = max(max(mx, max(Mf[t][c][0], Mf[t][c][1])), max(Mf[t][c][2], Mf[t][c][3]));
        if (__builtin_expect(__all(mx != 0xffffffffu) || spins > SPIN_CAP, 1)) break;
        __builtin_amdgcn_s_sleep(POLL_SLEEP);
#pragma unroll
        for (int t = 0; t < 2; ++t)
#pragma unroll
          for (int c = 0; c < 5; ++c) Mf[t][c] = LD16(lv, mo + (c * 20 + t + 3) * 1024);
      }
#pragma unroll
      for (int t = 3; t < 5; ++t)
#pragma unroll
        for (int mm = 0; mm < 4; ++mm)
#pragma unroll
          for (int c = 0; c < 5; ++c) acc[mm][c] = MFMA16(__builtin_bit_cast(bf16x8, Mf[t - 3][c]), P[t][mm], acc[mm][c]);
    }
    STAMP(1, 2);
    lds_wait(smem, 6, 4);
    {
      LAS char* p = smem + w * 20480 + lane * 16;
#pragma unroll
      for (int mm = 0; mm < 4; ++mm)
#pragma unroll
        for (int c = 0; c < 5; ++c) *(LAS f32x4*)(p + (mm * 5 + c) * 1024) = acc[mm][c];
    }
    BAR();
    return;
  } else {
    __builtin_amdgcn_s_setprio(3);
    const int lw = w - 4;
    u64 tag;
    {
      const unsigned xoff = (unsigned)((lane >> 3) * (D_ * 2) + (((lane & 7) ^ (lane >> 3)) << 4));
      const char* xg = (const char*)x + (size_t)(rb * 128 + 8 * lw) * (D_ * 2);
      const int ldw = cb <= 8 ? 1920 : F_;
      const int rl = lane >> 5, c = lane & 31;
      bool wact;
      int col;
      if (cb < 8) {
        const int cs = c - 2 * (cb & 1);
        col = c >= 24 ? 64 * cb + 8 * (c - 24) : (cs < 12 ? 640 + 80 * cb + 8 * cs : 1280 + 80 * cb + 8 * (cs - 12));
        wact = c >= 24 || (cs >= 0 && cs < 10) || (cs >= 12 && cs < 22);
      } else if (cb == 8) {
        const int wcc = c >= 14 ? 1 : 0, cc = c - 14 * wcc;
        col = 512 + 64 * wcc + 8 * cc;
        wact = c < 28 && cc < 8;
      } else {
        col = 224 * (cb - 9) + 8 * c;
        wact = c < 28 && col < F_;
      }
      if (!wact) col = 0;
      const unsigned woff = (unsigned)((rl * 16 * ldw + col) * 2);
      const char* wg = (const char*)(cb <= 8 ? wqkv : w1) + (size_t)lw * ldw * 2;
#define ROT1 1
      const int rot = ROT1 ? ((idx >> 2) + ((5 * (idx & 3)) >> 1) + 3 * xcd) % 10 : 0;
      auto stage = [&](int it, int buf) {
        int kt = it + rot; if (kt >= 10) kt -= 10;
        const unsigned sb = lds0 + buf * K1_STAGE;
        const char* xs = xg + kt * 128;
#pragma unroll
        for (int j = 0; j < 4; ++j) dma16(sb + (lw + 4 * j) * 1024, xoff, xs + j * (32 * D_ * 2));
        const char* wsrc = wg + (size_t)kt * 64 * ldw * 2;
        if (wact) {
#pragma unroll
          for (int j = 0; j < 8; ++j) {
            const int kj = 32 * (j >> 2) + 8 * (j & 1) + 4 * ((j >> 1) & 1);
            dma16(sb + K1_XB + (lw + 4 * j) * K1_WBLK, woff, wsrc + (size_t)kj * ldw * 2);
          }
        }
      };
      const int u2 = 4 * bid + lw, ks2 = u2 / 20, np2 = u2 - 20 * ks2;
      u32x4 w2a, w2b;
      {
        const u16* src = w2 + (size_t)(32 * (u2 < 960 ? ks2 : 0) + (lane >> 2)) * D_ + 32 * np2 + 8 * (lane & 3);
        asm volatile("global_load_dwordx4 %0, %2, off\n\tglobal_load_dwordx4 %1, %3, off"
                     : "=&v"(w2a), "=&v"(w2b) : "v"(src), "v"(src + 16 * D_) : "memory");
      }
      stage(0, 0);
      stage(1, 1);
      STAMP(0, 1);
      tag = load_tag(ws);
      asm volatile("s_waitcnt vmcnt(24)" : "+v"(w2a), "+v"(w2b) :: "memory");
      if (u2 < 960) {
        LAS char* t = smem + ASCR + lw * 3072;
        constexpr int RS = 96;
        *(LAS u32x4*)(t + (lane >> 2) * RS + (lane & 3) * 16) = w2a;
        *(LAS u32x4*)(t + (16 + (lane >> 2)) * RS + (lane & 3) * 16) = w2b;
        asm volatile("s_waitcnt lgkmcnt(0)" ::: "memory");
#pragma unroll
        for (int q = 0; q < 2; ++q) {
          const bf16x8 v = tr_frag(t + (4 * g + (l15 >> 2)) * RS + (l15 & 3) * 8, q * 32, 16 * RS);
          ST16(WS_W2F + ((2 * np2 + q) * 48 + ks2) * 1024 + lv, v);
        }
      }
    {
      const u32x4 sent = {0xffffffffu, 0xffffffffu, 0xffffffffu, 0xffffffffu};
      const int J = 32 * lw + idx, ks = xcd + 8 * (J / 40), gg = (J % 40) / 10, ct = J % 10, nl = 2 * (lane & 31) + (lane >> 5);
      if (J < 120 && ks < 20) ST16(WS_MFRAG + (((64 * ct + nl) >> 4) * 20 + ks) * 1024 + (16 * gg + (nl & 15)) * 16, sent);
      if (cb < 8 && lw < 2) {
        const unsigned kp = WS_KVP + rb * 51200 + (10 * (2 * cb + lw)) * 320;
#pragma unroll
        for (int q = 0; q < 3; ++q) ST16(kp + q * 1024 + lv, sent);
        if (lane < 8) ST16(kp + 3072 + lv, sent);
        if (lane < 10) ST16(WS_ZP + rb * 2560 + (10 * (2 * cb + lw)) * 16 + lv, sent);
      }
    }
      int nb = 2;
      u64 fe[4] = {0, 0, 0, 0};
#pragma unroll 1
      for (int i = 0; i < 8; ++i) {
        asm volatile("s_waitcnt vmcnt(12)" ::: "memory");
        __builtin_amdgcn_s_barrier();
        asm volatile("" ::: "memory");
        stage(i + 2, nb);
        nb = nb + 1; if (nb == 3) nb = 0;
        if (i == 2) arrive_publish(smem, 5, ws, WS_F0 + bid * 8, tag, 4, lane);
        if (DO_TOUCH && i == TOUCH_AT - 3 && lw == 0) {
          const char* fp = ws + WS_F0 + lane * 8;
          asm volatile("global_load_dwordx2 %0, %4, off sc1\n\tglobal_load_dwordx2 %1, %4, off offset:512 sc1\n\t"
                       "global_load_dwordx2 %2, %4, off offset:1024 sc1\n\tglobal_load_dwordx2 %3, %4, off offset:1536 sc1"
                       : "=&v"(fe[0]), "=&v"(fe[1]), "=&v"(fe[2]), "=&v"(fe[3]) : "v"(fp) : "memory");
        }
        if (DO_TOUCH && i == TOUCH_AT - 1 && lw == 0) {
          asm volatile("" : "+v"(fe[0]), "+v"(fe[1]), "+v"(fe[2]), "+v"(fe[3]));
          if (__all(fe[0] == tag && fe[1] == tag && fe[2] == tag && fe[3] == tag)) lds_signal(smem, 9, lane);
        }
      }
    }
    const int grow = rb * 128 + 8 * cb + 2 * lw;
    u32x4 gx0, gx1, gx2, gw0, gw1;
    {
      const u16* xr = x + (size_t)grow * D_;
      asm volatile("global_load_dwordx4 %0, %5, off\n\tglobal_load_dwordx4 %1, %5, off offset:1280\n\t"
                   "global_load_dwordx4 %2, %6, off\n\tglobal_load_dwordx4 %3, %7, off\n\tglobal_load_dwordx4 %4, %8, off"
                   : "=&v"(gx0), "=&v"(gx1), "=&v"(gx2), "=&v"(gw0), "=&v"(gw1)
                   : "v"(xr + 8 * lane), "v"(xr + ((lane >> 4) & 1) * D_ + 512 + 8 * (lane & 15)),
                     "v"(gate_w + 8 * lane), "v"(gate_w + 512 + 8 * (lane & 15)) : "memory");
    }
    asm volatile("s_waitcnt vmcnt(17)" ::: "memory");
    __builtin_amdgcn_s_barrier();
    asm volatile("" ::: "memory");
#define ROT2 1
    const int rot2 = ROT2 ? (3 * nu) >> 1 : 0;
    auto kso = [&](int s) { int k = s + rot2; return (unsigned)((k >= 12 ? k - 12 : k) * 1024); };
    const unsigned bo = WS_W2F + ((5 * nu) * 48 + 12 * lw) * 1024;
    auto slot_off = [&](int sl) { return sl < 3 ? RING1 + (3 * lw + sl) * 5120 : (3 * lw + sl - 3) * 5120; };
    auto fill = [&](int s, int sl) {
      const unsigned d = lds0 + slot_off(sl);
#pragma unroll
      for (int c = 0; c < 5; ++c) dma16_sc1(d + c * 1024, lv, ws + bo + c * 48 * 1024 + kso(s));
    };
    for (unsigned spins = 0;; ++spins) {
      u64 f0[4];
#pragma unroll
      for (int q = 0; q < 4; ++q) f0[q] = __hip_atomic_load((GAS u64*)(ws + WS_F0) + 64 * q + lane, __ATOMIC_RELAXED, __HIP_MEMORY_SCOPE_AGENT);
      if (__all(f0[0] == tag && f0[1] == tag && f0[2] == tag && f0[3] == tag) || spins > SPIN_CAP) break;
      __builtin_amdgcn_s_sleep(POLL_SLEEP);
    }
    asm volatile("s_waitcnt vmcnt(0)" ::: "memory");
    lds_signal(smem, 1, lane);
    __builtin_amdgcn_s_barrier();
    asm volatile("" ::: "memory");
    STAMP(0, 2);
    __builtin_amdgcn_s_setprio(2);
#define LDH(dst, soff) asm volatile("buffer_load_dwordx4 %0, %1, %2, %3 offen sc1" : "=v"(dst) : "v"(lv), "s"(wsr), "s"((unsigned)(soff)) : "memory")
    u32x4 Br[3][5];
#pragma unroll
    for (int s = 0; s < 3; ++s) fill(s, s);
    asm volatile("" : "+v"(gx0), "+v"(gx1), "+v"(gx2), "+v"(gw0), "+v"(gw1));
    {
      float s0 = 0.f, s1 = 0.f, s2 = 0.f;
#pragma unroll
      for (int j = 0; j < 4; ++j) {
        s0 += rbf(bflo(gx0[j]) * bflo(gw0[j])) + rbf(bfhi(gx0[j]) * bfhi(gw0[j]));
        s1 += rbf(bflo(gx1[j]) * bflo(gw0[j])) + rbf(bfhi(gx1[j]) * bfhi(gw0[j]));
        s2 += rbf(bflo(gx2[j]) * bflo(gw1[j])) + rbf(bfhi(gx2[j]) * bfhi(gw1[j]));
      }
      if (lane < 16) s0 += s2;
      else if (lane < 32) s1 += s2;
#pragma unroll
      for (int o = 32; o >= 1; o >>= 1) {
        s0 += __shfl_xor(s0, o);
        s1 += __shfl_xor(s1, o);
      }
      if (lane < 2) {
        float l = rbf(lane == 0 ? s0 : s1);
        float gv = rbf(1.f / (1.f + __expf(-l)));
        __builtin_amdgcn_raw_buffer_store_b32(__float_as_uint(gv), wsr, WS_G + (grow + lane) * 4, 0, 16);
      }
    }
    SCHED();
#pragma unroll
    for (int s = 0; s < 3; ++s)
#pragma unroll
      for (int c = 0; c < 5; ++c) LDH(Br[s][c], bo + c * 48 * 1024 + kso(6 + s));
    waitv<15>();
    arrive_publish(smem, 8, ws, WS_FG + (rb * 16 + cb) * 8, tag, 4, lane);
    lds_wait(smem, 2, 4);
#pragma unroll
    for (int s = 3; s < 6; ++s) fill(s, s);
    STAMP(0, 3);
    DRAIN();
#pragma unroll
    for (int s = 0; s < 3; ++s)
      asm volatile("" : "+v"(Br[s][0]), "+v"(Br[s][1]), "+v"(Br[s][2]), "+v"(Br[s][3]), "+v"(Br[s][4]));
    STAMP(0, 4);
    STAMP(0, 7);
    __builtin_amdgcn_s_setprio(1);
    wait_flags(ws, WS_F1 + ((rb2 >> 1) * 16 + 9 + (24 * lw + (lane < 24 ? lane : 0)) / 14) * 8, true, tag);
    STAMP(0, 5);
    u32x4 A[WIN][4];
    const unsigned a0 = ap + 12 * lw * 1024;
#pragma unroll
    for (int s = 0; s < WIN; ++s)
#pragma unroll
      for (int m = 0; m < 4; ++m) LDH(A[s][m], a0 + m * NKS * 1024 + kso(s));
#define FSTEP(s) do { \
      waitv<nwait(s)>(); \
      asm volatile("" : "+v"(A[(s) % WIN][0]), "+v"(A[(s) % WIN][1]), "+v"(A[(s) % WIN][2]), "+v"(A[(s) % WIN][3])); \
      bf16x8 B[5]; \
      if ((s) >= 6 && (s) < 9) { _Pragma("unroll") for (int c = 0; c < 5; ++c) B[c] = __builtin_bit_cast(bf16x8, Br[(s) >= 6 && (s) < 9 ? (s) - 6 : 0][c]); } \
      else { LAS char* sl = smem + slot_off((s) < 6 ? (s) : (s) - 9) + lv; \
             _Pragma("unroll") for (int c = 0; c < 5; ++c) B[c] = *(LAS bf16x8*)(sl + c * 1024); } \
      _Pragma("unroll") for (int c = 0; c < 5; ++c) \
        _Pragma("unroll") for (int m = 0; m < 4; ++m) \
          acc[m][c] = MFMA16(B[c], __builtin_bit_cast(bf16x8, A[(s) % WIN][m]), (s) == 0 ? ((f32x4){0.f, 0.f, 0.f, 0.f}) : acc[m][c]); \
      SCHED(); \
      if ((s) < 3) { asm volatile("s_waitcnt lgkmcnt(0)" ::: "memory"); fill((s) + 9, (s)); } \
      if ((s) == 5) lds_signal(smem, 6, lane); \
      if ((s) + WIN < 12) { _Pragma("unroll") for (int m = 0; m < 4; ++m) LDH(A[(s) % WIN][m], a0 + m * NKS * 1024 + kso((s) + WIN)); } \
      SCHED(); \
    } while (0)
    FSTEP(0); FSTEP(1); FSTEP(2); FSTEP(3); FSTEP(4); FSTEP(5);
    FSTEP(6); FSTEP(7); FSTEP(8); FSTEP(9); FSTEP(10); FSTEP(11);
    STAMP(0, 6);
    tagf = tag;
    if (bid == 0 && lw == 0) {
#pragma unroll
      for (int i = 0; i < 4; ++i) {
        wait_flags(ws, WS_F1 + (64 * i + lane) * 8, true, tag);
      }
      if (lane == 0) __hip_atomic_store((GAS u64*)(ws + WS_EPOCH), tag, __ATOMIC_RELAXED, __HIP_MEMORY_SCOPE_AGENT);
    }
  }
  {
    const int f = w - 4;
    asm volatile("s_waitcnt lgkmcnt(0)" ::: "memory");
#pragma unroll
    for (int m = 0; m < 4; ++m) {
      if (m != f) {
        LAS char* p = smem + RING1 + (3 * f + (m < f ? m : m - 1)) * 5120 + lv;
#pragma unroll
        for (int c = 0; c < 5; ++c) *(LAS f32x4*)(p + c * 1024) = acc[m][c];
      }
    }
    f32x4 fb[5];
#pragma unroll
    for (int m = 0; m < 4; ++m)
      if (m == f) {
#pragma unroll
        for (int c = 0; c < 5; ++c) fb[c] = acc[m][c];
      }
    const int row = 64 * rb2 + 16 * f + l15;
    wait_flags(ws, WS_FG + ((rb2 >> 1) * 16 + 8 * (rb2 & 1) + (lane & 7)) * 8, true, tagf);
    const float gv = __uint_as_float(__builtin_amdgcn_raw_buffer_load_b32(wsr, WS_G + row * 4, 0, 16));
    u32x2 bb[5];
#pragma unroll
    for (int c = 0; c < 5; ++c) bb[c] = *(const u32x2*)(b2 + 80 * nu + 16 * c + 4 * g);
    BAR();
    STAMP(1, 4);
    const float om = rbf(1.f - gv);
#pragma unroll
    for (int c = 0; c < 5; ++c) {
      LAS char* pa = smem + (f * 5 + c) * 1024 + lv;
      const f32x4 fa = (*(LAS f32x4*)(pa) + *(LAS f32x4*)(pa + 20480)) + (*(LAS f32x4*)(pa + 2 * 20480) + *(LAS f32x4*)(pa + 3 * 20480));
#pragma unroll
      for (int o = 1; o < 4; ++o) {
        const int src = (f + o) & 3;
        fb[c] += *(LAS f32x4*)(smem + RING1 + (3 * src + (f < src ? f : f - 1)) * 5120 + c * 1024 + lv);
      }
      const int n = 80 * nu + 16 * c + 4 * g;
      const unsigned b01 = pack2(fb[c][0], fb[c][1]), b23 = pack2(fb[c][2], fb[c][3]);
      const unsigned y01 = pack2(bflo(b01) + bflo(bb[c][0]), bfhi(b01) + bfhi(bb[c][0]));
      const unsigned y23 = pack2(bflo(b23) + bflo(bb[c][1]), bfhi(b23) + bfhi(bb[c][1]));
      const unsigned a01 = pack2(fa[0], fa[1]), a23 = pack2(fa[2], fa[3]);
      const unsigned ga01 = pack2(gv * bflo(a01), gv * bfhi(a01)), ga23 = pack2(gv * bflo(a23), gv * bfhi(a23));
      const unsigned gb01 = pack2(om * bflo(y01), om * bfhi(y01)), gb23 = pack2(om * bflo(y23), om * bfhi(y23));
      u32x2 v = {pack2(bflo(ga01) + bflo(gb01), bfhi(ga01) + bfhi(gb01)), pack2(bflo(ga23) + bflo(gb23), bfhi(ga23) + bfhi(gb23))};
      *(u32x2*)(out + (size_t)row * D_ + n) = v;
    }
  }
  STAMP(1, 5);
}

extern "C" void kernel_launch(void* const* d_in, const int* in_sizes, int n_in, void* d_out, int out_size,
                              void* d_ws, size_t ws_size, hipStream_t stream) {
  const u16* x = (const u16*)d_in[0];
  const u16* wqkv = (const u16*)d_in[1];
  const u16* wo = (const u16*)d_in[2];
  const u16* w1 = (const u16*)d_in[4];
  const u16* b1 = (const u16*)d_in[5];
  const u16* w2 = (const u16*)d_in[6];
  const u16* b2 = (const u16*)d_in[7];
  const u16* gw = (const u16*)d_in[8];
  (void)hipFuncSetAttribute((const void*)fused_kernel, hipFuncAttributeMaxDynamicSharedMemorySize, LDS_BYTES);
  fused_kernel<<<256, 512, LDS_BYTES, stream>>>(x, wqkv, wo, w1, b1, w2, b2, gw, (char*)d_ws, (u16*)d_out);
}
```

```cpp
#include <hip/hip_runtime.h>
#include <hip/hip_bf16.h>
#include <stdint.h>

typedef __attribute__((ext_vector_type(8))) short bf16x8;
typedef __attribute__((ext_vector_type(4))) short s16x4;
typedef __attribute__((ext_vector_type(4))) float f32x4;
typedef __attribute__((ext_vector_type(2))) float f32x2;
typedef __attribute__((ext_vector_type(2))) __bf16 bf16x2v;
typedef __attribute__((ext_vector_type(4))) unsigned u32x4;
typedef __attribute__((ext_vector_type(2))) unsigned u32x2;
typedef unsigned short u16;
#define LAS __attribute__((address_space(3)))
#define GAS __attribute__((address_space(1)))
#define DI __device__ __forceinline__

constexpr int S_ = 2048, D_ = 640, F_ = 1536;
constexpr int NKS = 68;

constexpr size_t WS_AFRAG = 4096;
constexpr size_t WS_BFRAG = WS_AFRAG + (size_t)128 * NKS * 1024;
constexpr size_t WS_KVP = WS_BFRAG + (size_t)40 * NKS * 1024;
constexpr size_t WS_ZP = WS_KVP + (size_t)16 * 51200;
constexpr size_t WS_G = WS_ZP + 16 * 2560;
constexpr size_t WS_END = WS_G + 2048 * 4;

DI unsigned pack2(float a, float b) {
  f32x2 f = {a, b};
  return __builtin_bit_cast(unsigned, __builtin_convertvector(f, bf16x2v));
}
DI float rbf(float a) { return __uint_as_float(pack2(a, a) << 16); }
DI float bflo(unsigned u) { return __uint_as_float(u << 16); }
DI float bfhi(unsigned u) { return __uint_as_float(u & 0xffff0000u); }
typedef __attribute__((ext_vector_type(2))) short s16x2;
DI unsigned phi2(float a0, float a1) {
  const unsigned p = pack2(a0, a1);
  const float t0 = bflo(p), t1 = bfhi(p);
  const float e0 = __builtin_amdgcn_exp2f(fminf(t0 * 1.44269504f, 0.f));
  const float e1 = __builtin_amdgcn_exp2f(fminf(t1 * 1.44269504f, 0.f));
  return pack2((fmaxf(t0, 0.f) + e0) + 1e-6f, (fmaxf(t1, 0.f) + e1) + 1e-6f);
}
DI f32x4 sel4(bool c, f32x4 a, f32x4 b) {
  f32x4 r;
#pragma unroll
  for (int i = 0; i < 4; ++i) { float x = a[i], y = b[i]; asm volatile("" : "+v"(x), "+v"(y)); r[i] = c ? x : y; }
  return r;
}
DI unsigned sqrelu2(float a0, float a1) {
  unsigned q = pack2(a0, a1);
  const s16x2 z = {0, 0};
  q = __builtin_bit_cast(unsigned, __builtin_elementwise_max(__builtin_bit_cast(s16x2, q), z));
  const float u0 = bflo(q), u1 = bfhi(q);
  return pack2(u0 * u0, u1 * u1);
}
#define MFMA16(a, b, c) __builtin_amdgcn_mfma_f32_16x16x32_bf16((a), (b), (c), 0, 0, 0)
#define SCHED() __builtin_amdgcn_sched_barrier(0)
#define SGB(mask, n) __builtin_amdgcn_sched_group_barrier(mask, n, 0)
#define INTERLEAVE() do { _Pragma("unroll") for (int q_ = 0; q_ < 18; ++q_) { SGB(0x100, 1); SGB(0x8, 1); } SGB(0x8, 10); } while (0)
#define ST16(p, v) asm volatile("global_store_dwordx4 %0, %1, off sc1\n\ts_nop 1" :: "v"((char*)(p)), "v"(v) : "memory")
#define ST8(p, v) asm volatile("global_store_dwordx2 %0, %1, off sc1\n\ts_nop 1" :: "v"((char*)(p)), "v"(v) : "memory")

constexpr int K1_XB = 16384;
constexpr int K1_WBLK = 1056;
constexpr int K1_WGRP = 8 * K1_WBLK;
constexpr int K1_WB = 32 * K1_WBLK;
constexpr int K1_STAGE = K1_XB + K1_WB;
constexpr int K1_SCR = 3 * K1_STAGE;
constexpr int K1_LDS = K1_SCR + 4 * 3072;

DI void dma16(unsigned lds_off, unsigned voff, const char* gbase) {
  const unsigned lo = __builtin_amdgcn_readfirstlane((unsigned)(uintptr_t)gbase);
  const unsigned hi = __builtin_amdgcn_readfirstlane((unsigned)((uintptr_t)gbase >> 32));
  const unsigned long long gb = ((unsigned long long)hi << 32) | lo;
  const unsigned lo2 = __builtin_amdgcn_readfirstlane(lds_off);
  asm volatile("s_mov_b32 m0, %0\n\ts_nop 3\n\tglobal_load_lds_dwordx4 %1, %2"
               :: "s"(lo2), "v"(voff), "s"(gb) : "memory");
}

DI bf16x8 tr_frag(LAS char* p0, int off) {
  s16x4 lo = __builtin_amdgcn_ds_read_tr16_b64_v4i16((LAS s16x4*)(p0 + off));
  s16x4 hi = __builtin_amdgcn_ds_read_tr16_b64_v4i16((LAS s16x4*)(p0 + off + K1_WGRP));
  return __builtin_shufflevector(lo, hi, 0, 1, 2, 3, 4, 5, 6, 7);
}

template <bool KV>
DI void k1_compute(const u16* __restrict__ b1, char* __restrict__ ws, LAS char* smem, int rb, int cb, int w) {
  const int lane = threadIdx.x & 63;
  const int wr = w >> 1, wc = w & 1;
  const int g = lane >> 4, l15 = lane & 15;

  f32x4 acc[7][4];
#pragma unroll
  for (int t = 0; t < 7; ++t) {
    f32x4 init = {0.f, 0.f, 0.f, 0.f};
    if (!KV) {
      int T = 14 * (cb - 9) + 7 * wc + t;
      T = T < 0 ? 0 : (T > 95 ? 95 : T);
      const u32x2 bb = *(const u32x2*)(b1 + 16 * T + 4 * g);
      if (cb > 8) init = (f32x4){bflo(bb[0]), bfhi(bb[0]), bflo(bb[1]), bfhi(bb[1])};
    }
#pragma unroll
    for (int m = 0; m < 4; ++m) acc[t][m] = init;
  }
  const int xrd = (wr * 64 + l15) * 128;
  const int xsw = l15 & 7;
  const int wrow = K1_XB + (4 * (g & 1) + (l15 >> 2)) * K1_WBLK + (g >> 1) * 512;
  const int ka = 32 * (cb & 1);
  const int wrd = wrow + (KV ? ka + wc * 80 : wc * 224) + (l15 & 3) * 8;
  const int wrdq = wrow + 384 + wc * 64 + (l15 & 3) * 8;
  const int wrd4 = wrow + ka + wc * 80 + 32 + (l15 & 1) * 40 + ((l15 >> 1) & 1) * 192;

  bf16x8 a0[4], b0[7], a1[4], b1f[7];
  auto rd = [&](LAS char* sb, int s, bf16x8 (&a)[4], bf16x8 (&b)[7]) {
#pragma unroll
    for (int m = 0; m < 4; ++m) a[m] = *(LAS bf16x8*)(sb + xrd + m * 2048 + (((4 * s + g) ^ xsw) << 4));
    if (KV) {
      b[0] = tr_frag(sb + wrd, s * 2 * K1_WGRP + 0);
      b[1] = tr_frag(sb + wrd, s * 2 * K1_WGRP + 40);
      b[2] = tr_frag(sb + wrd, s * 2 * K1_WGRP + 192);
      b[3] = tr_frag(sb + wrd, s * 2 * K1_WGRP + 232);
      b[4] = tr_frag(sb + wrd4, s * 2 * K1_WGRP);
      b[5] = tr_frag(sb + wrdq, s * 2 * K1_WGRP);
      b[6] = tr_frag(sb + wrdq, s * 2 * K1_WGRP + 32);
    } else {
#pragma unroll
      for (int t = 0; t < 7; ++t) b[t] = tr_frag(sb + wrd, s * 2 * K1_WGRP + 32 * t);
    }
  };
  auto mm = [&](bf16x8 (&a)[4], bf16x8 (&b)[7]) {
#pragma unroll
    for (int t = 0; t < 7; ++t)
#pragma unroll
      for (int m = 0; m < 4; ++m) {
        if (KV && t < 5) acc[t][m] = MFMA16(a[m], b[t], acc[t][m]);
        else acc[t][m] = MFMA16(b[t], a[m], acc[t][m]);
      }
  };
  __builtin_amdgcn_s_barrier();
  asm volatile("" ::: "memory");
  rd(smem, 0, a0, b0);
  rd(smem, 1, a1, b1f);
  mm(a0, b0);
  int buf = 1;
#pragma unroll 1
  for (int i = 1; i < 10; ++i) {
    asm volatile("s_waitcnt lgkmcnt(0)" ::: "memory");
    __builtin_amdgcn_s_barrier();
    asm volatile("" ::: "memory");
    LAS char* sb = smem + buf * K1_STAGE;
    SCHED();
    rd(sb, 0, a0, b0);
    mm(a1, b1f);
    INTERLEAVE();
    SCHED();
    rd(sb, 1, a1, b1f);
    mm(a0, b0);
    INTERLEAVE();
    SCHED();
    buf = buf + 1; if (buf == 3) buf = 0;
  }
  __builtin_amdgcn_s_setprio(3);
  mm(a1, b1f);

  char* afrag = ws + WS_AFRAG + ((size_t)(rb * 8 + wr * 4) * NKS) * 1024 + lane * 16;
  f32x4 R[10];
#pragma unroll
  for (int t = 0; t < 10; ++t) R[t] = (f32x4){0.f, 0.f, 0.f, 0.f};
  const int hp = 2 * cb + wc;
  const int T0 = 14 * (cb - 9) + 7 * wc;
#pragma unroll 1
  for (int u = 0; u < 2; ++u) {
    if (KV) {
      const bool iskl = l15 < 8;
      const bf16x8 ones = {0x3f80, 0x3f80, 0x3f80, 0x3f80, 0x3f80, 0x3f80, 0x3f80, 0x3f80};
      bf16x8 T[5];
#pragma unroll
      for (int t = 0; t < 5; ++t) {
        u32x4 p;
#pragma unroll
        for (int j = 0; j < 4; ++j) {
          const float e0 = acc[t][j >> 1][2 * (j & 1)], e1 = acc[t][j >> 1][2 * (j & 1) + 1];
          if (t < 2) p[j] = phi2(e0, e1);
          else if (t < 4) p[j] = pack2(e0, e1);
          else p[j] = iskl ? phi2(e0, e1) : pack2(e0, e1);
        }
        T[t] = __builtin_bit_cast(bf16x8, p);
      }
      R[0] = MFMA16(T[0], T[2], R[0]);
      R[1] = MFMA16(T[0], T[4], R[1]);
      R[2] = MFMA16(T[4], T[2], R[2]);
      R[3] = MFMA16(T[1], T[3], R[3]);
      R[4] = MFMA16(T[1], T[4], R[4]);
      R[5] = MFMA16(T[4], T[3], R[5]);
      R[6] = MFMA16(T[4], T[4], R[6]);
      R[7] = MFMA16(T[0], ones, R[7]);
      R[8] = MFMA16(T[1], ones, R[8]);
      R[9] = MFMA16(T[4], ones, R[9]);
#pragma unroll
      for (int m = 0; m < 2; ++m) {
        u32x4 v;
        v[0] = phi2(acc[5][m][0], acc[5][m][1]);
        v[1] = phi2(acc[5][m][2], acc[5][m][3]);
        v[2] = phi2(acc[6][m][0], acc[6][m][1]);
        v[3] = phi2(acc[6][m][2], acc[6][m][3]);
        ST16(afrag + ((size_t)m * NKS + 48 + hp) * 1024, v);
      }
    } else if (cb == 8) {
#pragma unroll
      for (int pp = 0; pp < 2; ++pp)
#pragma unroll
        for (int m = 0; m < 2; ++m) {
          u32x4 v;
          v[0] = phi2(acc[2 * pp][m][0], acc[2 * pp][m][1]);
          v[1] = phi2(acc[2 * pp][m][2], acc[2 * pp][m][3]);
          v[2] = phi2(acc[2 * pp + 1][m][0], acc[2 * pp + 1][m][1]);
          v[3] = phi2(acc[2 * pp + 1][m][2], acc[2 * pp + 1][m][3]);
          ST16(afrag + ((size_t)m * NKS + 48 + 16 + 2 * wc + pp) * 1024, v);
        }
    } else {
      const bool full = T0 + 6 < 96;
#pragma unroll
      for (int m = 0; m < 2; ++m) {
        u32x2 hv[7];
#pragma unroll
        for (int t = 0; t < 7; ++t) {
          hv[t][0] = sqrelu2(acc[t][m][0], acc[t][m][1]);
          hv[t][1] = sqrelu2(acc[t][m][2], acc[t][m][3]);
        }
        char* bm = afrag + ((size_t)m * NKS + (T0 >> 1)) * 1024;
        if (wc == 0) {
#pragma unroll
          for (int pp = 0; pp < 3; ++pp) {
            u32x4 v = {hv[2 * pp][0], hv[2 * pp][1], hv[2 * pp + 1][0], hv[2 * pp + 1][1]};
            ST16(bm + pp * 1024, v);
          }
          ST8(bm + 3 * 1024, hv[6]);
        } else {
          ST8(bm + 8, hv[0]);
#pragma unroll
          for (int pp = 0; pp < 3; ++pp) {
            u32x4 v = {hv[2 * pp + 1][0], hv[2 * pp + 1][1], hv[2 * pp + 2][0], hv[2 * pp + 2][1]};
            if (pp < 2 || full) ST16(bm + (pp + 1) * 1024, v);
          }
        }
      }
    }
#pragma unroll
    for (int t = 0; t < 7; ++t) { acc[t][0] = acc[t][2]; acc[t][1] = acc[t][3]; }
    afrag += (size_t)2 * NKS * 1024;
  }
  if (KV) {
    LAS char* red = smem + K1_STAGE + wc * 10240;
    if (wr == 1) {
#pragma unroll
      for (int t = 0; t < 10; ++t) *(LAS f32x4*)(red + t * 1024 + lane * 16) = R[t];
    }
    asm volatile("s_waitcnt lgkmcnt(0)" ::: "memory");
    __builtin_amdgcn_s_barrier();
    asm volatile("" ::: "memory");
    if (wr == 0) {
#pragma unroll
      for (int t = 0; t < 10; ++t) R[t] += *(LAS f32x4*)(red + t * 1024 + lane * 16);
      char* kp = ws + WS_KVP + (size_t)rb * 51200 + (size_t)(10 * hp) * 320;
      ST16(kp + g * 320 + l15 * 16, R[0]);
      ST16(kp + 1600 + g * 320 + l15 * 16, R[3]);
      if (l15 >= 8) { const f32x4 v_ = sel4(l15 >= 12, R[4], R[1]); ST16(kp + (l15 >= 12 ? 1600 : 0) + g * 320 + (16 + (l15 & 3)) * 16, v_); }
      if (g < 2) { const f32x4 v_ = sel4(g != 0, R[5], R[2]); ST16(kp + g * 1600 + 4 * 320 + l15 * 16, v_); }
      if (g < 2 && (l15 >> 2) == 2 + g) ST16(kp + g * 1600 + 4 * 320 + (16 + (l15 & 3)) * 16, R[6]);
      char* zp = ws + WS_ZP + (size_t)rb * 2560 + (size_t)(10 * hp) * 16;
      if (l15 < 2 || (l15 == 2 && g < 2)) {
        const f32x4 v_ = sel4(l15 == 0, R[7], sel4(l15 == 1, R[8], R[9]));
        ST16(zp + (l15 == 0 ? g * 16 : (l15 == 1 ? 80 + g * 16 : g * 80 + 64)), v_);
      }
    }
  }
}

__global__ void __launch_bounds__(512, 2)
k1_kernel(const u16* __restrict__ x, const u16* __restrict__ wqkv, const u16* __restrict__ w1,
          const u16* __restrict__ b1, const u16* __restrict__ w2, const u16* __restrict__ gate_w,
          char* __restrict__ ws) {
  extern __shared__ __attribute__((aligned(16))) char smem_[];
  LAS char* smem = (LAS char*)smem_;
  const int bid = blockIdx.x, tid = threadIdx.x, lane = tid & 63;
  const int w = __builtin_amdgcn_readfirstlane(tid >> 6);
  const int xcd = bid & 7, idx = bid >> 3;
  const int rb = 8 * (xcd >> 2) + (idx >> 2);
  const int cb = (xcd & 3) + 4 * (idx & 3);

  if (w < 4) {
    if (cb < 8) k1_compute<true>(b1, ws, smem, rb, cb, w);
    else k1_compute<false>(b1, ws, smem, rb, cb, w);
    return;
  }
  __builtin_amdgcn_s_setprio(3);
  const int lw = w - 4;
  const unsigned lds0 = (unsigned)(size_t)smem;
  const unsigned xoff = (unsigned)((lane >> 3) * (D_ * 2) + (((lane & 7) ^ (lane >> 3)) << 4));
  const char* xg = (const char*)x + (size_t)(rb * 128 + 8 * lw) * (D_ * 2);
  const int ldw = cb <= 8 ? 1920 : F_;
  const int rl = lane >> 5, c = lane & 31;
  bool wact;
  int col;
  if (cb < 8) {
    const int cs = c - 2 * (cb & 1);
    col = c >= 24 ? 64 * cb + 8 * (c - 24) : (cs < 12 ? 640 + 80 * cb + 8 * cs : 1280 + 80 * cb + 8 * (cs - 12));
    wact = c >= 24 || (cs >= 0 && cs < 10) || (cs >= 12 && cs < 22);
  } else if (cb == 8) {
    const int wcc = c >= 14 ? 1 : 0, cc = c - 14 * wcc;
    col = 512 + 64 * wcc + 8 * cc;
    wact = c < 28 && cc < 8;
  } else {
    col = 224 * (cb - 9) + 8 * c;
    wact = c < 28 && col < F_;
  }
  if (!wact) col = 0;
  const unsigned woff = (unsigned)((rl * 16 * ldw + col) * 2);
  const char* wg = (const char*)(cb <= 8 ? wqkv : w1) + (size_t)lw * ldw * 2;

  constexpr int NSH = 2;
  const int rot = ((idx >> 2) + 2 * (idx & 3) + 3 * xcd) % (10 - NSH);
  auto stage = [&](int it, int buf) {
    int kt = it + rot; if (kt >= 10) kt -= 10 - NSH;
    if (it < NSH) kt = it;
    const unsigned sb = lds0 + buf * K1_STAGE;
    const char* xs = xg + kt * 128;
#pragma unroll
    for (int j = 0; j < 4; ++j) dma16(sb + (lw + 4 * j) * 1024, xoff, xs + j * (32 * D_ * 2));
    const char* wsrc = wg + (size_t)kt * 64 * ldw * 2;
    if (wact) {
#pragma unroll
      for (int j = 0; j < 8; ++j) {
        const int kj = 32 * (j >> 2) + 8 * (j & 1) + 4 * ((j >> 1) & 1);
        dma16(sb + K1_XB + (lw + 4 * j) * K1_WBLK, woff, wsrc + (size_t)kj * ldw * 2);
      }
    }
  };
  stage(0, 0);
  stage(1, 1);
  asm volatile("s_waitcnt vmcnt(12)" ::: "memory");
  __builtin_amdgcn_s_barrier();
  asm volatile("" ::: "memory");
  stage(2, 2);

  int nb = 0;
#pragma unroll 1
  for (int i = 1; i < 8; ++i) {
    asm volatile("s_waitcnt vmcnt(12)" ::: "memory");
    __builtin_amdgcn_s_barrier();
    asm volatile("" ::: "memory");
    stage(i + 2, nb);
    nb = nb + 1; if (nb == 3) nb = 0;
  }
  asm volatile("s_waitcnt vmcnt(12)" ::: "memory");
  __builtin_amdgcn_s_barrier();
  asm volatile("s_waitcnt vmcnt(0)" ::: "memory");
  __builtin_amdgcn_s_barrier();
  asm volatile("" ::: "memory");
  __builtin_amdgcn_s_setprio(0);
  const int u2 = 4 * bid + lw;
  const int ks2 = u2 / 20, np2 = u2 - 20 * ks2;
  u32x4 w2a = {0, 0, 0, 0}, w2b = {0, 0, 0, 0};
  if (u2 < 960) {
    const u16* src = w2 + (size_t)(32 * ks2 + (lane >> 2)) * D_ + 32 * np2 + 8 * (lane & 3);
    w2a = *(const u32x4*)src;
    w2b = *(const u32x4*)(src + 16 * D_);
  }
  const int grow = rb * 128 + 8 * cb + 2 * lw;
  u32x4 gx0 = *(const u32x4*)(x + (size_t)grow * D_ + 8 * lane);
  u32x4 gx1 = *(const u32x4*)(x + (size_t)(grow + 1) * D_ + 8 * lane);
  u32x4 gw0 = *(const u32x4*)(gate_w + 8 * lane);
  u32x4 gx2 = *(const u32x4*)(x + (size_t)(grow + ((lane >> 4) & 1)) * D_ + 512 + 8 * (lane & 15));
  u32x4 gw1 = *(const u32x4*)(gate_w + 512 + 8 * (lane & 15));
  {
    const u32x4 sent = {0xffffffffu, 0xffffffffu, 0xffffffffu, 0xffffffffu};
    if (u2 < 800) {
      const int nt = u2 / 20, ks = u2 - 20 * nt;
      ST16(ws + WS_BFRAG + ((size_t)nt * NKS + 48 + ks) * 1024 + lane * 16, sent);
    }
  }

  {
    float s0 = 0.f, s1 = 0.f, s2 = 0.f;
#pragma unroll
    for (int j = 0; j < 4; ++j) {
      s0 += rbf(bflo(gx0[j]) * bflo(gw0[j])) + rbf(bfhi(gx0[j]) * bfhi(gw0[j]));
      s1 += rbf(bflo(gx1[j]) * bflo(gw0[j])) + rbf(bfhi(gx1[j]) * bfhi(gw0[j]));
      s2 += rbf(bflo(gx2[j]) * bflo(gw1[j])) + rbf(bfhi(gx2[j]) * bfhi(gw1[j]));
    }
    if (lane < 16) s0 += s2;
    else if (lane < 32) s1 += s2;
#pragma unroll
    for (int o = 32; o >= 1; o >>= 1) {
      s0 += __shfl_xor(s0, o);
      s1 += __shfl_xor(s1, o);
    }
    if (lane < 2) {
      float l = rbf(lane == 0 ? s0 : s1);
      float gv = rbf(1.f / (1.f + __expf(-l)));
      ((float*)(ws + WS_G))[grow + lane] = gv;
    }
  }
  if (u2 < 960) {
    LAS char* t = smem + K1_SCR + lw * 3072;
    constexpr int RS = 96;
    *(LAS u32x4*)(t + (lane >> 2) * RS + (lane & 3) * 16) = w2a;
    *(LAS u32x4*)(t + (16 + (lane >> 2)) * RS + (lane & 3) * 16) = w2b;
    asm volatile("s_waitcnt lgkmcnt(0)" ::: "memory");
    const int g = lane >> 4, l15 = lane & 15;
#pragma unroll
    for (int i = 0; i < 2; ++i) {
      LAS char* p = t + (4 * g + (l15 >> 2)) * RS + i * 32 + (l15 & 3) * 8;
      s16x4 lo = __builtin_amdgcn_ds_read_tr16_b64_v4i16((LAS s16x4*)p);
      s16x4 hi = __builtin_amdgcn_ds_read_tr16_b64_v4i16((LAS s16x4*)(p + 16 * RS));
      bf16x8 v = __builtin_shufflevector(lo, hi, 0, 1, 2, 3, 4, 5, 6, 7);
      ST16(ws + WS_BFRAG + ((size_t)(2 * np2 + i) * NKS + ks2) * 1024 + lane * 16, v);
    }
  }
  if (cb < 8) __builtin_amdgcn_s_barrier();
}

constexpr int K2_LDS = 8 * 20 * 1024;
constexpr int WIN = 3;
constexpr int APRIO = 1;

__global__ void __launch_bounds__(512, 2)
k2_kernel(const u16* __restrict__ wo, const u16* __restrict__ b2, char* ws, u16* __restrict__ out) {
  extern __shared__ __attribute__((aligned(16))) char smem_[];
  LAS char* smem = (LAS char*)smem_;
  const int bid = blockIdx.x, tid = threadIdx.x, lane = tid & 63;
  const int w = __builtin_amdgcn_readfirstlane(tid >> 6);
  const int g = lane >> 4, l15 = lane & 15;
  const int xcd = bid & 7, idx = bid >> 3;
  const int rb = 8 * (xcd >> 1) + (idx >> 2);
  const int nu = 4 * (xcd & 1) + (idx & 3);
  const __amdgpu_buffer_rsrc_t wsr = __builtin_amdgcn_make_buffer_rsrc(ws, 0, (int)WS_END, 0x00020000);
  const unsigned lv = lane * 16;
  const unsigned ap = (unsigned)(WS_AFRAG + (size_t)(4 * rb) * NKS * 1024);
#define LDF(soff) __builtin_bit_cast(bf16x8, __builtin_amdgcn_raw_buffer_load_b128(wsr, lv, (soff), 0))
  if (w < 4) {
    f32x4 acc[4][5];
#pragma unroll
    for (int m = 0; m < 4; ++m)
#pragma unroll
      for (int c = 0; c < 5; ++c) acc[m][c] = (f32x4){0.f, 0.f, 0.f, 0.f};
    bf16x8 A[WIN][4], B[WIN][5];
    const unsigned a0 = ap + 12 * w * 1024;
    const unsigned b0 = (unsigned)(WS_BFRAG + (size_t)(5 * nu) * NKS * 1024) + 12 * w * 1024;
#pragma unroll
    for (int s = 0; s < WIN; ++s) {
      const int so = s;
#pragma unroll
      for (int m = 0; m < 4; ++m) A[s][m] = LDF(a0 + (m * NKS + so) * 1024);
#pragma unroll
      for (int c = 0; c < 5; ++c) B[s][c] = LDF(b0 + (c * NKS + so) * 1024);
    }
    SCHED();
#pragma unroll
    for (int s = 0; s < 12; ++s) {
#pragma unroll
      for (int m = 0; m < 4; ++m)
#pragma unroll
        for (int c = 0; c < 5; ++c) acc[m][c] = MFMA16(B[s % WIN][c], A[s % WIN][m], acc[m][c]);
      SCHED();
      if (s + WIN < 12) {
        const int so = s + WIN;
#pragma unroll
        for (int m = 0; m < 4; ++m) A[s % WIN][m] = LDF(a0 + (m * NKS + so) * 1024);
#pragma unroll
        for (int c = 0; c < 5; ++c) B[s % WIN][c] = LDF(b0 + (c * NKS + so) * 1024);
      }
      SCHED();
    }
    LAS char* p = smem + w * 20480 + lane * 16;
#pragma unroll
    for (int m = 0; m < 4; ++m)
#pragma unroll
      for (int c = 0; c < 5; ++c) *(LAS f32x4*)(p + (m * 5 + c) * 1024) = acc[m][c];
  } else {
    __builtin_amdgcn_s_setprio(3);
    const int aw = w - 4;
    const int J = 32 * aw + idx;
    const int ksj = xcd + 8 * (J / 40);
    const bool job = J < 120 && ksj < 20;
    bf16x8 P[5][4];
    const unsigned a0 = ap + (48 + 5 * aw) * 1024;
    f32x4 zq[16];
    const unsigned zo = (unsigned)((40 * aw + (lane < 40 ? lane : 39)) * 16);
#define LOADZP() _Pragma("unroll") for (int r = 0; r < 16; ++r) \
      zq[r] = __builtin_bit_cast(f32x4, __builtin_amdgcn_raw_buffer_load_b128(wsr, zo, (unsigned)(WS_ZP + (size_t)r * 2560), 0))
    if (job) {
      const int ks = ksj;
      const int rem = J % 40, gg = rem / 10, ct = rem - 10 * gg;
      const int hf = lane >> 5, l31 = lane & 31;
      const int m = 8 * ks + 4 * hf + gg, ph = m / 5, dq = m - 5 * ph;
      LAS float* skv = (LAS float*)(smem + w * 20480);
      const unsigned kvo = (unsigned)(m * 320 + (l31 < 20 ? l31 : 19) * 16);
      f32x4 t[16];
#pragma unroll
      for (int r = 0; r < 16; ++r)
        t[r] = __builtin_bit_cast(f32x4, __builtin_amdgcn_raw_buffer_load_b128(wsr, kvo, (unsigned)(WS_KVP + (size_t)r * 51200), 0));
      unsigned wv[20];
#pragma unroll
      for (int e = 0; e < 20; ++e) wv[e] = *(const unsigned*)(wo + (size_t)(20 * ph + e) * D_ + 64 * ct + 2 * l31);
      SCHED();
      const f32x4 s = (((t[0] + t[1]) + (t[2] + t[3])) + ((t[4] + t[5]) + (t[6] + t[7]))) +
                      (((t[8] + t[9]) + (t[10] + t[11])) + ((t[12] + t[13]) + (t[14] + t[15])));
      if (l31 < 20) {
        const unsigned q0 = pack2(s[0], s[1]), q1 = pack2(s[2], s[3]);
        *(LAS f32x4*)(skv + hf * 80 + l31 * 4) = (f32x4){bflo(q0), bfhi(q0), bflo(q1), bfhi(q1)};
      }
      asm volatile("s_waitcnt lgkmcnt(0)" ::: "memory");
      f32x4 o0 = {0.f, 0.f, 0.f, 0.f}, o1 = {0.f, 0.f, 0.f, 0.f};
#pragma unroll
      for (int e = 0; e < 20; ++e) {
        f32x4 kvv = *(LAS f32x4*)(skv + hf * 80 + e * 4);
        o0 += kvv * bflo(wv[e]);
        o1 += kvv * bfhi(wv[e]);
      }
      const auto rx = __builtin_amdgcn_permlane32_swap(pack2(o0[0], o0[1]), pack2(o1[0], o1[1]), false, false);
      const auto ry = __builtin_amdgcn_permlane32_swap(pack2(o0[2], o0[3]), pack2(o1[2], o1[3]), false, false);
      const u32x4 v = {rx[0], ry[0], rx[1], ry[1]};
      const int n = 64 * ct + 2 * l31 + hf;
      __builtin_amdgcn_raw_buffer_store_b128(v, wsr, (unsigned)(WS_BFRAG + ((size_t)(n >> 4) * NKS + 48 + ks) * 1024 + (16 * gg + (n & 15)) * 16), 0, 16);
    }
    SCHED();
    LOADZP();
#pragma unroll
    for (int tt = 0; tt < 5; ++tt)
#pragma unroll
      for (int mm = 0; mm < 4; ++mm) P[tt][mm] = LDF(a0 + (mm * NKS + tt) * 1024);
    SCHED();
    __builtin_amdgcn_s_setprio(APRIO);
    u32x4 Mf[3][5];
    const unsigned mo = (unsigned)(WS_BFRAG + ((size_t)(5 * nu) * NKS + 48 + 5 * aw) * 1024);
    u32x4 Z[5];
    {
      const f32x4 zs = (((zq[0] + zq[1]) + (zq[2] + zq[3])) + ((zq[4] + zq[5]) + (zq[6] + zq[7]))) +
                       (((zq[8] + zq[9]) + (zq[10] + zq[11])) + ((zq[12] + zq[13]) + (zq[14] + zq[15])));
      LAS char* zl = smem + w * 20480 + 1024;
      if (lane < 40) *(LAS u32x2*)(zl + (((lane >> 3) * 4 + (lane & 3)) * 2 + ((lane >> 2) & 1)) * 8) = (u32x2){pack2(zs[0], zs[1]), pack2(zs[2], zs[3])};
      asm volatile("s_waitcnt lgkmcnt(0)" ::: "memory");
#pragma unroll
      for (int t = 0; t < 5; ++t) Z[t] = *(LAS u32x4*)(zl + (t * 4 + g) * 16);
    }
    {
      f32x4 Dn[4][3];
#pragma unroll
      for (int sg = 0; sg < 3; ++sg) {
        const int hr = ((l15 >> 2) + 4 * (4 * sg + (l15 & 3))) / 5;
#pragma unroll
        for (int m = 0; m < 4; ++m) Dn[m][sg] = (f32x4){0.f, 0.f, 0.f, 0.f};
#pragma unroll
        for (int t = 0; t < 5; ++t) {
          const bool used = (sg == 0 && t <= 2) || (sg == 1 && t >= 1) || (sg == 2 && t >= 3);
          if (used) {
            const bool k0 = ((8 * t + g) / 5) == hr, k1 = ((8 * t + 4 + g) / 5) == hr;
            u32x4 v = {k0 ? Z[t][0] : 0u, k0 ? Z[t][1] : 0u, k1 ? Z[t][2] : 0u, k1 ? Z[t][3] : 0u};
            const bf16x8 zm = __builtin_bit_cast(bf16x8, v);
#pragma unroll
            for (int m = 0; m < 4; ++m) Dn[m][sg] = MFMA16(zm, P[t][m], Dn[m][sg]);
          }
        }
      }
#pragma unroll
      for (int m = 0; m < 4; ++m) {
#pragma unroll
        for (int t = 0; t < 5; ++t) {
          u32x4 p = __builtin_bit_cast(u32x4, P[t][m]);
          u32x4 q;
          const float r0 = __builtin_amdgcn_rcpf(Dn[m][(2 * t) >> 2][(2 * t) & 3] + 1e-6f);
          const float r1 = __builtin_amdgcn_rcpf(Dn[m][(2 * t + 1) >> 2][(2 * t + 1) & 3] + 1e-6f);
          q[0] = pack2(bflo(p[0]) * r0, bfhi(p[0]) * r0);
          q[1] = pack2(bflo(p[1]) * r0, bfhi(p[1]) * r0);
          q[2] = pack2(bflo(p[2]) * r1, bfhi(p[2]) * r1);
          q[3] = pack2(bflo(p[3]) * r1, bfhi(p[3]) * r1);
          P[t][m] = __builtin_bit_cast(bf16x8, q);
          asm volatile("" : "+v"(P[t][m]));
        }
      }
    }
    SCHED();
    __builtin_amdgcn_s_setprio(2);
#pragma unroll
    for (int t = 0; t < 3; ++t)
#pragma unroll
      for (int c = 0; c < 5; ++c) Mf[t][c] = __builtin_amdgcn_raw_buffer_load_b128(wsr, lv, mo + (c * NKS + t) * 1024, 16);
    SCHED();
    f32x4 acc[4][5];
    for (unsigned spins = 0;; ++spins) {
      unsigned mx = 0u;
#pragma unroll
      for (int t = 0; t < 3; ++t)
#pragma unroll
        for (int c = 0; c < 5; ++c) mx = max(max(mx, max(Mf[t][c][0], Mf[t][c][1])), max(Mf[t][c][2], Mf[t][c][3]));
      if (__builtin_expect(__all(mx != 0xffffffffu) || spins > (1u << 16), 1)) break;
      __builtin_amdgcn_s_sleep(32);
#pragma unroll
      for (int t = 0; t < 3; ++t)
#pragma unroll
        for (int c = 0; c < 5; ++c) Mf[t][c] = __builtin_amdgcn_raw_buffer_load_b128(wsr, lv, mo + (c * NKS + t) * 1024, 16);
    }
#pragma unroll
    for (int t = 0; t < 3; ++t) {
#pragma unroll
      for (int m = 0; m < 4; ++m)
#pragma unroll
        for (int c = 0; c < 5; ++c)
          acc[m][c] = MFMA16(__builtin_bit_cast(bf16x8, Mf[t][c]), P[t][m], t == 0 ? ((f32x4){0.f, 0.f, 0.f, 0.f}) : acc[m][c]);
      SCHED();
      if (t < 2) {
#pragma unroll
        for (int c = 0; c < 5; ++c) Mf[t][c] = __builtin_amdgcn_raw_buffer_load_b128(wsr, lv, mo + (c * NKS + t + 3) * 1024, 16);
      }
      SCHED();
    }
    for (unsigned spins = 0;; ++spins) {
      unsigned mx = 0u;
#pragma unroll
      for (int t = 0; t < 2; ++t)
#pragma unroll
        for (int c = 0; c < 5; ++c) mx = max(max(mx, max(Mf[t][c][0], Mf[t][c][1])), max(Mf[t][c][2], Mf[t][c][3]));
      if (__builtin_expect(__all(mx != 0xffffffffu) || spins > (1u << 16), 1)) break;
      __builtin_amdgcn_s_sleep(32);
#pragma unroll
      for (int t = 0; t < 2; ++t)
#pragma unroll
        for (int c = 0; c < 5; ++c) Mf[t][c] = __builtin_amdgcn_raw_buffer_load_b128(wsr, lv, mo + (c * NKS + t + 3) * 1024, 16);
    }
#pragma unroll
    for (int t = 3; t < 5; ++t) {
#pragma unroll
      for (int m = 0; m < 4; ++m)
#pragma unroll
        for (int c = 0; c < 5; ++c) acc[m][c] = MFMA16(__builtin_bit_cast(bf16x8, Mf[t - 3][c]), P[t][m], acc[m][c]);
    }
    LAS char* p = smem + w * 20480 + lane * 16;
#pragma unroll
    for (int m = 0; m < 4; ++m)
#pragma unroll
      for (int c = 0; c < 5; ++c) *(LAS f32x4*)(p + (m * 5 + c) * 1024) = acc[m][c];
  }
  const int fm = w >> 1, c0 = (w & 1) ? 3 : 0, nc = (w & 1) ? 2 : 3;
  const int row = 64 * rb + 16 * fm + l15;
  const float gv = ((const float*)(ws + WS_G))[row];
  u32x2 bb[3];
#pragma unroll
  for (int i = 0; i < 3; ++i) bb[i] = *(const u32x2*)(b2 + 80 * nu + 16 * (c0 + (i < nc ? i : 0)) + 4 * g);
  __syncthreads();
  {
    const float om = rbf(1.f - gv);
#pragma unroll
    for (int i = 0; i < 3; ++i) {
      if (i < nc) {
        const int c = c0 + i;
        LAS char* p = smem + (fm * 5 + c) * 1024 + lane * 16;
        f32x4 fb = (*(LAS f32x4*)(p) + *(LAS f32x4*)(p + 20480)) + (*(LAS f32x4*)(p + 2 * 20480) + *(LAS f32x4*)(p + 3 * 20480));
        f32x4 fa = (*(LAS f32x4*)(p + 4 * 20480) + *(LAS f32x4*)(p + 5 * 20480)) + (*(LAS f32x4*)(p + 6 * 20480) + *(LAS f32x4*)(p + 7 * 20480));
        const int n = 80 * nu + 16 * c + 4 * g;
        const unsigned b01 = pack2(fb[0], fb[1]), b23 = pack2(fb[2], fb[3]);
        const unsigned y01 = pack2(bflo(b01) + bflo(bb[i][0]), bfhi(b01) + bfhi(bb[i][0]));
        const unsigned y23 = pack2(bflo(b23) + bflo(bb[i][1]), bfhi(b23) + bfhi(bb[i][1]));
        const unsigned a01 = pack2(fa[0], fa[1]), a23 = pack2(fa[2], fa[3]);
        const unsigned ga01 = pack2(gv * bflo(a01), gv * bfhi(a01)), ga23 = pack2(gv * bflo(a23), gv * bfhi(a23));
        const unsigned gb01 = pack2(om * bflo(y01), om * bfhi(y01)), gb23 = pack2(om * bflo(y23), om * bfhi(y23));
        u32x2 v = {pack2(bflo(ga01) + bflo(gb01), bfhi(ga01) + bfhi(gb01)), pack2(bflo(ga23) + bflo(gb23), bfhi(ga23) + bfhi(gb23))};
        *(u32x2*)(out + (size_t)row * D_ + n) = v;
      }
    }
  }
}

extern "C" void kernel_launch(void* const* d_in, const int* in_sizes, int n_in, void* d_out, int out_size,
                              void* d_ws, size_t ws_size, hipStream_t stream) {
  const u16* x = (const u16*)d_in[0];
  const u16* wqkv = (const u16*)d_in[1];
  const u16* wo = (const u16*)d_in[2];
  const u16* w1 = (const u16*)d_in[4];
  const u16* b1 = (const u16*)d_in[5];
  const u16* w2 = (const u16*)d_in[6];
  const u16* b2 = (const u16*)d_in[7];
  const u16* gw = (const u16*)d_in[8];
  char* ws = (char*)d_ws;
  (void)hipFuncSetAttribute((const void*)k1_kernel, hipFuncAttributeMaxDynamicSharedMemorySize, K1_LDS);
  (void)hipFuncSetAttribute((const void*)k2_kernel, hipFuncAttributeMaxDynamicSharedMemorySize, K2_LDS);
  k1_kernel<<<256, 512, K1_LDS, stream>>>(x, wqkv, w1, b1, w2, gw, ws);
  k2_kernel<<<256, 512, K2_LDS, stream>>>(wo, b2, ws, (u16*)d_out);
}
```
